```python
import torch

def _softmax(x, axis=-1):
    return torch.softmax(x, dim=axis)
def _gelu(x):
    return torch.nn.functional.gelu(x, approximate="tanh")
def _silu(x):
    return torch.nn.functional.silu(x)
def _sigmoid(x):
    return torch.sigmoid(x)
def _layernorm(x, axis=-1, eps=1e-5):
    m = x.mean(dim=axis, keepdim=True)
    v = x.var(dim=axis, keepdim=True, unbiased=False)
    return (x - m) / torch.sqrt(v + eps)
def _scatter_add(tgt, idx, src, axis=0):
    return tgt.clone().index_add_(axis, idx, src)
def _segment_sum(src, seg, n):
    out = torch.zeros((n,) + src.shape[1:], dtype=src.dtype)
    return out.index_add_(0, seg, src)
def _pad(x, pw):
    flat = tuple(v for pair in reversed(pw) for v in pair)
    return torch.nn.functional.pad(x, flat)

kernel_name = 'parallel_sum__mha_block__dense_activation_mish__prompt'

def setup_inputs():

    _w = (lambda a: (lambda t: t.to(torch.bfloat16) if t.is_floating_point() else t)(torch.as_tensor(a)))
    def _A_setup():
        B, S, D, H = 1, 2048, 512, 32
        return {
            "x": _w((lambda *s: torch.randn(*s, dtype=torch.bfloat16) * 0.05)(B, S, D)),
            "wqkv": _w((lambda *s: torch.randn(*s, dtype=torch.bfloat16) * 0.05)(D, 3 * D)),
            "wo": _w((lambda *s: torch.randn(*s, dtype=torch.bfloat16) * 0.05)(D, D)),
        }
    _A_in = _A_setup()

    def _B_setup():
        B, S, D, F = 1, 2048, 512, 1792
        return {
            "x": _w((lambda *s: torch.randn(*s, dtype=torch.bfloat16) * 0.05)(B, S, D)),
            "w1": _w((lambda *s: torch.randn(*s, dtype=torch.bfloat16) * 0.8)(D, F) * (D ** -0.5)),
            "b1": _w((lambda *s: torch.randn(*s, dtype=torch.bfloat16) * 0.8)(F)),
            "w2": _w((lambda *s: torch.randn(*s, dtype=torch.bfloat16) * 0.2)(F, D) * (F ** -0.5)),
        }
    _B_in = _B_setup()

    return {
        "A_x": _A_in["x"],
        "A_wqkv": _A_in["wqkv"],
        "A_wo": _A_in["wo"],
        "B_x": _B_in["x"],
        "B_w1": _B_in["w1"],
        "B_b1": _B_in["b1"],
        "B_w2": _B_in["w2"],
    }

def reference(A_x, A_wqkv, A_wo, B_x, B_w1, B_b1, B_w2):
    def _A_ref(x, wqkv, wo):
        B, S, D = x.shape
        H = 32
        Dh = D // H
        qkv = (lambda x, *d: x.permute(*d))((x @ wqkv).reshape(B, S, 3, H, Dh), 2, 0, 3, 1, 4)
        q, k, v = qkv[0], qkv[1], qkv[2]
        scores = torch.einsum("bhqd,bhkd->bhqk", q, k) * (Dh ** -0.5)
        p = _softmax(scores, axis=-1)
        o = torch.einsum("bhqk,bhkd->bhqd", p, v)
        return (lambda x, *d: x.permute(*d))(o, 0, 2, 1, 3).reshape(B, S, D) @ wo

    def _B_ref(x, w1, b1, w2):
        h = torch.einsum("bsd,df->bsf", x, w1) + b1
        sp = torch.log1p(torch.exp(-torch.abs(h))) + torch.where(h > 0, h, torch.zeros_like(h))
        act = h * torch.tanh(sp)
        return torch.einsum("bsf,fd->bsd", act, w2)

    x = A_x
    xn = _layernorm(x, axis=-1)
    return x + _A_ref(xn, A_wqkv, A_wo) + _B_ref(xn, B_w1, B_b1, B_w2)
```

```cpp
#include <hip/hip_runtime.h>
#include <hip/hip_bf16.h>
typedef unsigned short u16;
__device__ inline float bf2f(u16 v){ return __uint_as_float(((unsigned)v)<<16); }
__device__ inline u16 f2bf(float f){ unsigned u=__float_as_uint(f); u += 0x7fff + ((u>>16)&1); return (u16)(u>>16); }
constexpr int S=2048, D=512, H=32, DH=16, F=1792;
__global__ void ln_k(const u16* x, float* xn){
  int r=blockIdx.x; __shared__ float red[2][8];
  float v[2]; float s=0;
  for(int i=0;i<2;i++){ v[i]=bf2f(x[r*D+threadIdx.x+256*i]); s+=v[i]; }
  for(int o=32;o;o>>=1) s+=__shfl_xor(s,o);
  if((threadIdx.x&63)==0) red[0][threadIdx.x>>6]=s; __syncthreads();
  float m=(red[0][0]+red[0][1]+red[0][2]+red[0][3])/D;
  float q=0; for(int i=0;i<2;i++){ q+=(v[i]-m)*(v[i]-m);}
  for(int o=32;o;o>>=1) q+=__shfl_xor(q,o);
  if((threadIdx.x&63)==0) red[1][threadIdx.x>>6]=q; __syncthreads();
  float var=(red[1][0]+red[1][1]+red[1][2]+red[1][3])/D;
  float rs=rsqrtf(var+1e-5f);
  for(int i=0;i<2;i++) xn[r*D+threadIdx.x+256*i]=(v[i]-m)*rs;
}
template<int MODE>
__global__ void gemm_k(const float* A, const u16* W, const u16* bias, float* C, int N, int K){
  int n=blockIdx.x*256+threadIdx.x; int m=blockIdx.y; if(n>=N) return;
  float acc=0; for(int k=0;k<K;k++) acc+=A[m*K+k]*bf2f(W[k*N+n]);
  if(MODE==1){ float h=acc+bf2f(bias[n]); float sp=log1pf(expf(-fabsf(h)))+fmaxf(h,0.f); acc=h*tanhf(sp);}
  C[m*N+n]=acc;
}
__global__ void attn_k(const float* qkv, float* o){
  int h=blockIdx.y; int q=blockIdx.x*256+threadIdx.x;
  float qv[DH]; for(int d=0;d<DH;d++) qv[d]=qkv[q*3*D+h*DH+d]*0.25f;
  float m=-1e30f,l=0,acc[DH]; for(int d=0;d<DH;d++) acc[d]=0;
  for(int k=0;k<S;k++){ float s=0; for(int d=0;d<DH;d++) s+=qv[d]*qkv[k*3*D+D+h*DH+d];
    float mn=fmaxf(m,s); float a=expf(m-mn), p=expf(s-mn); l=l*a+p; for(int d=0;d<DH;d++) acc[d]=acc[d]*a+p*qkv[k*3*D+2*D+h*DH+d]; m=mn; }
  for(int d=0;d<DH;d++) o[q*D+h*DH+d]=acc[d]/l;
}
__global__ void fin_k(const u16* x, const float* a, const float* b, u16* out){ int i=blockIdx.x*256+threadIdx.x; out[i]=f2bf(bf2f(x[i])+a[i]+b[i]); }
extern "C" void kernel_launch(void* const* d_in, const int* in_sizes, int n_in, void* d_out, int out_size, void* d_ws, size_t ws_size, hipStream_t stream) {
  const u16* x=(const u16*)d_in[0]; const u16* wqkv=(const u16*)d_in[1]; const u16* wo=(const u16*)d_in[2];
  const u16* w1=(const u16*)d_in[4]; const u16* b1=(const u16*)d_in[5]; const u16* w2=(const u16*)d_in[6];
  float* xn=(float*)d_ws; float* qkv=xn+S*D; float* act=qkv+S*3*D; float* o=act+S*F; float* ya=o+S*D; float* yb=ya+S*D;
  ln_k<<<S,256,0,stream>>>(x,xn);
  gemm_k<0><<<dim3(6,S),256,0,stream>>>(xn,wqkv,nullptr,qkv,3*D,D);
  gemm_k<1><<<dim3(7,S),256,0,stream>>>(xn,w1,b1,act,F,D);
  attn_k<<<dim3(S/256,H),256,0,stream>>>(qkv,o);
  gemm_k<0><<<dim3(2,S),256,0,stream>>>(o,wo,nullptr,ya,D,D);
  gemm_k<0><<<dim3(2,S),256,0,stream>>>(act,w2,nullptr,yb,D,F);
  fin_k<<<S*D/256,256,0,stream>>>(x,ya,yb,(u16*)d_out);
}
```

```cpp
#include <hip/hip_runtime.h>
#include <stdint.h>

typedef __attribute__((ext_vector_type(8))) __bf16 bf16x8;
typedef __attribute__((ext_vector_type(2))) __bf16 bf16x2;
typedef __attribute__((ext_vector_type(2))) float f32x2;
typedef __attribute__((ext_vector_type(4))) float f32x4;
typedef __attribute__((ext_vector_type(16))) float f32x16;
typedef __attribute__((ext_vector_type(2))) unsigned u32x2;
typedef __attribute__((ext_vector_type(4))) unsigned u32x4;
typedef __attribute__((ext_vector_type(4))) short s16x4;
typedef unsigned long long u64;
typedef __attribute__((address_space(1))) u64 gu64;
typedef __amdgpu_buffer_rsrc_t rsrc_t;
#define AS3 __attribute__((address_space(3)))
#define DI __device__ __forceinline__

constexpr unsigned WS_EPOCH = 0;
constexpr unsigned WS_QKFLAG = 4096;
constexpr unsigned WS_ACTFLAG = WS_QKFLAG + 8192;
constexpr unsigned WS_OFLAG = WS_ACTFLAG + 8192;
constexpr unsigned WS_STAMP = 65536;
constexpr unsigned WS_Q = 1u << 20;
constexpr unsigned WS_K = WS_Q + (2u << 20);
constexpr unsigned WS_V = WS_K + (2u << 20);
constexpr unsigned WS_O = WS_V + (2u << 20);
constexpr unsigned WS_ACT = WS_O + (2u << 20);
constexpr unsigned WS_W2F = WS_ACT + 64u * 112u * 1024u;
constexpr unsigned WS_END = WS_W2F + 144u * 16u * 1024u;

constexpr unsigned SLOT = 32768;
constexpr unsigned L_K = 0, L_V = 65536, L_ONES = 131072, L_Q = 135168;
constexpr unsigned L_PART = 65536, PART_ROW = 272, PART_SZ = 64 * PART_ROW;
constexpr unsigned L_MISC = 159744;
constexpr unsigned M_CSQK = L_MISC, M_CSV = M_CSQK + 256, M_CSF = M_CSV + 256,
                   M_CNT = M_CSF + 512, M_CNT2 = M_CNT + 4, M_GO = M_CNT + 8, M_ZEND = M_CNT + 16,
                   M_RS = L_MISC + 2048, M_MR = M_RS + 512, M_B1 = M_MR + 512, M_TOK = M_B1 + 512;
constexpr unsigned LDS_BYTES = 163840;

constexpr unsigned SPIN_CAP = 40000;
#define PUBSTAGE 1

#define MFMA32(a, b, c) __builtin_amdgcn_mfma_f32_32x32x16_bf16(a, b, c, 0, 0, 0)
#define MFMA16(a, b, c) __builtin_amdgcn_mfma_f32_16x16x32_bf16(a, b, c, 0, 0, 0)
#define SBAR() __builtin_amdgcn_sched_barrier(0)
#define PIN(x) asm volatile("" : "+v"(x))
#define BAR() asm volatile("s_waitcnt lgkmcnt(0)\n\ts_barrier" ::: "memory")
#define WAITL0() __builtin_amdgcn_s_waitcnt(0xC07F)
#define BAR_RAW() asm volatile("s_barrier" ::: "memory")
#define WAITV(n) asm volatile("s_waitcnt vmcnt(" #n ")" ::: "memory")
#define RLX_AGENT __ATOMIC_RELAXED, __HIP_MEMORY_SCOPE_AGENT
#define EX2(x) __builtin_amdgcn_exp2f(x)

#define STAMP(i) do { } while (0)
#define LSTAMP(i) do { } while (0)
#define LFLUSH() do { } while (0)

template <typename T> DI T lds_ld(unsigned a) { return *(const AS3 T*)(uintptr_t)a; }
template <typename T> DI void lds_st(unsigned a, T v) { *(AS3 T*)(uintptr_t)a = v; }
DI bf16x8 lds_tr2(unsigned a0, unsigned a1) {
  s16x4 lo = __builtin_amdgcn_ds_read_tr16_b64_v4i16((AS3 s16x4*)(uintptr_t)a0);
  s16x4 hi = __builtin_amdgcn_ds_read_tr16_b64_v4i16((AS3 s16x4*)(uintptr_t)a1);
  return __builtin_bit_cast(bf16x8, __builtin_shufflevector(lo, hi, 0, 1, 2, 3, 4, 5, 6, 7));
}
DI unsigned pk2(float a, float b) { f32x2 v = {a, b}; return __builtin_bit_cast(unsigned, __builtin_convertvector(v, bf16x2)); }
DI float bflo(unsigned u) { return __uint_as_float(u << 16); }
DI float bfhi(unsigned u) { return __uint_as_float(u & 0xffff0000u); }
DI float half_sum(float v) { auto r = __builtin_amdgcn_permlane32_swap(__float_as_uint(v), __float_as_uint(v), false, false); return __uint_as_float(r[0]) + __uint_as_float(r[1]); }
DI float half_max(float v) { auto r = __builtin_amdgcn_permlane32_swap(__float_as_uint(v), __float_as_uint(v), false, false); return fmaxf(__uint_as_float(r[0]), __uint_as_float(r[1])); }
DI unsigned rfl(unsigned v) { return __builtin_amdgcn_readfirstlane(v); }
DI void dma(rsrc_t r, unsigned voff, unsigned soff, unsigned ldsdst) {
  asm volatile("s_mov_b32 m0, %0\n\ts_nop 0\n\tbuffer_load_dwordx4 %1, %2, %3 offen lds" ::"s"(ldsdst), "v"(voff), "s"(r), "s"(soff) : "memory");
}
DI void dma_sc1(rsrc_t r, unsigned voff, unsigned soff, unsigned ldsdst) {
  asm volatile("s_mov_b32 m0, %0\n\ts_nop 0\n\tbuffer_load_dwordx4 %1, %2, %3 offen sc1 lds" ::"s"(ldsdst), "v"(voff), "s"(r), "s"(soff) : "memory");
}
DI void st_sc1(u32x4 v, rsrc_t r, unsigned voff, unsigned soff) { __builtin_amdgcn_raw_buffer_store_b128(v, r, voff, soff, 16); }
DI u32x4 ld_sc1(rsrc_t r, unsigned voff, unsigned soff) { return __builtin_amdgcn_raw_buffer_load_b128(r, voff, soff, 16); }
DI void publish(char* ws, unsigned off, u64 tok, int lane) {
  WAITV(0);
  if (lane == 0) __hip_atomic_store((gu64*)(ws + off), tok, RLX_AGENT);
}
DI void poll1(char* ws, unsigned off, u64 tok) {
#pragma nounroll
  for (unsigned spins = 0; spins < SPIN_CAP; ++spins) {
    const u64 a = __hip_atomic_load((gu64*)(ws + off), RLX_AGENT);
    if (__all(a == tok)) break;
    __builtin_amdgcn_s_sleep(2);
  }
  asm volatile("" ::: "memory");
}
DI u64 lds_tok() { const u32x2 t = lds_ld<u32x2>(M_TOK); return ((u64)rfl(t[1]) << 32) | rfl(t[0]); }

DI void ld_rows(float (&d)[16], unsigned base, int h) {
#pragma unroll
  for (int g = 0; g < 4; ++g) {
    const f32x4 v = lds_ld<f32x4>(base + 16 * h + 32 * g);
    d[4 * g] = v[0]; d[4 * g + 1] = v[1]; d[4 * g + 2] = v[2]; d[4 * g + 3] = v[3];
  }
}

DI void publish_wg(char* ws, unsigned off, unsigned cnt, u64 tok, int lane) {
  WAITV(0);
  if (lane == 0) {
    const unsigned old = __hip_atomic_fetch_add((AS3 unsigned*)(uintptr_t)cnt, 1u, __ATOMIC_RELAXED, __HIP_MEMORY_SCOPE_WORKGROUP);
    if (old == 7) __hip_atomic_store((gu64*)(ws + off), tok, RLX_AGENT);
  }
}

template <int MODE>
DI void gemm_pass(int lane, unsigned xb, unsigned wb, int ct, int pr, f32x16& a0, f32x16& a1, f32x4& cs, float& sx, float& sxx,
                  char* ws = nullptr, unsigned flag = 0, u64 tok = 0) {
  const int r = lane & 31, h = lane >> 5, q4 = (lane & 15) >> 2;
  unsigned xa[4];
#pragma unroll
  for (int j = 0; j < 4; ++j) xa[j] = xb + r * 128 + (((h ^ ((r >> 1) & 7)) ^ (2 * (j ^ pr))) << 4);
  const unsigned wl = wb + (8 * h + q4) * 128 + ((lane >> 4) & 1) * 32 + (lane & 3) * 8 + ((ct ^ (q4 >> 1)) << 6);
  unsigned wA = wl + pr * 2048, wB = wl + (pr ^ 1) * 2048, uA = wA ^ 64, uB = wB ^ 64;
  const unsigned one2 = ((lane & 15) == ((lane >> 4) & 1)) ? 0x3f803f80u : 0u;
  const bf16x8 sel = __builtin_bit_cast(bf16x8, (u32x4){one2, one2, one2, one2});
  bf16x8 xP[4], yP[4], wP[4], xQ[4], yQ[4], wQ[4];
  float sx1 = 0.f, sxx1 = 0.f;
#define RDX(X, j) X[j] = lds_ld<bf16x8>(xa[j])
#define RDW(W, j) W[j] = lds_tr2((((j) & 1) ? wB : wA) + ((j) >> 1) * 4096, (((j) & 1) ? wB : wA) + ((j) >> 1) * 4096 + 512)
#define RDY(Y, j) do { if (MODE == 0) Y[j] = lds_tr2((((j) & 1) ? uB : uA) + ((j) >> 1) * 4096, (((j) & 1) ? uB : uA) + ((j) >> 1) * 4096 + 512); \
                       if (MODE == 2) Y[j] = lds_ld<bf16x8>(xa[j] + 4096); } while (0)
#define RDALL(X, Y, W, j) do { RDX(X, j); RDW(W, j); RDY(Y, j); } while (0)
#define QST(X, i) do { const unsigned u_ = __builtin_bit_cast(u32x4, X)[i]; const float l_ = bflo(u_), h_ = bfhi(u_); \
    sx += l_; sx1 += h_; sxx = __builtin_fmaf(l_, l_, sxx); sxx1 = __builtin_fmaf(h_, h_, sxx1); PIN(sx); PIN(sx1); PIN(sxx); PIN(sxx1); } while (0)
#define MM0(X, Y, W, j) do { if (MODE == 1) a0 = MFMA32(X[j], W[j], a0); else a0 = MFMA32(W[j], X[j], a0); SBAR(); } while (0)
#define MM1(X, Y, W, j) do { if (MODE == 0) a1 = MFMA32(Y[j], X[j], a1); if (MODE == 2) a1 = MFMA32(W[j], Y[j], a1); \
    if (MODE == 1) { QST(X[j], 0); QST(X[j], 1); QST(X[j], 2); QST(X[j], 3); } SBAR(); } while (0)
#define MMC(W, j) do { cs = MFMA16(sel, W[j], cs); SBAR(); } while (0)
#define GSTAGE(X, Y, W, XN, YN, WN, RD, d) do { \
    WAITL0(); SBAR(); \
    if (RD) { xa[0] += d; xa[1] += d; xa[2] += d; xa[3] += d; wA += d; wB += d; if (MODE == 0) { uA += d; uB += d; } SBAR(); BAR_RAW(); SBAR(); } \
    MM0(X, Y, W, 0); if (RD) RDALL(XN, YN, WN, 0); SBAR(); MM1(X, Y, W, 0); if (RD) RDALL(XN, YN, WN, 1); SBAR(); \
    MM0(X, Y, W, 1); if (RD) RDALL(XN, YN, WN, 2); SBAR(); MM1(X, Y, W, 1); if (RD) RDALL(XN, YN, WN, 3); SBAR(); \
    MMC(W, 0); MM0(X, Y, W, 2); MM1(X, Y, W, 2); MMC(W, 2); MM0(X, Y, W, 3); MM1(X, Y, W, 3); } while (0)
  RDALL(xP, yP, wP, 0); RDALL(xP, yP, wP, 1); RDALL(xP, yP, wP, 2); RDALL(xP, yP, wP, 3); SBAR();
#pragma nounroll
  for (int s = 0; s < 3; ++s) {
    GSTAGE(xP, yP, wP, xQ, yQ, wQ, true, (int)SLOT);
    const int d2 = (s == 1) ? -3 * (int)SLOT : (int)SLOT;
    GSTAGE(xQ, yQ, wQ, xP, yP, wP, true, d2);
    if (MODE == 2 && s == PUBSTAGE) publish_wg(ws, flag, M_CNT, tok, lane);
  }
  GSTAGE(xP, yP, wP, xQ, yQ, wQ, true, (int)SLOT);
  GSTAGE(xQ, yQ, wQ, xP, yP, wP, false, 0);
  if (MODE == 1) { sx += sx1; sxx += sxx1; }
#undef RDX
#undef RDW
#undef RDY
#undef RDALL
#undef QST
#undef MM0
#undef MM1
#undef MMC
#undef GSTAGE
}
DI void cs_add(unsigned dst, const f32x4& cs, int lane) {
  if (lane < 16) {
    __hip_atomic_fetch_add((AS3 float*)(uintptr_t)(dst + lane * 4), cs[0], __ATOMIC_RELAXED, __HIP_MEMORY_SCOPE_WORKGROUP);
    __hip_atomic_fetch_add((AS3 float*)(uintptr_t)(dst + 64 + lane * 4), cs[1], __ATOMIC_RELAXED, __HIP_MEMORY_SCOPE_WORKGROUP);
  }
}
DI f32x2 mish2(f32x2 z) {
  f32x2 u; u[0] = EX2(z[0]); u[1] = EX2(z[1]);
  u += 1.0f;
  const f32x2 d = __builtin_elementwise_fma(u, u, (f32x2){1.0f, 1.0f});
  f32x2 rc; rc[0] = __builtin_amdgcn_rcpf(d[0]); rc[1] = __builtin_amdgcn_rcpf(d[1]);
  return z * __builtin_elementwise_fma(rc, (f32x2){-1.3862943611198906f, -1.3862943611198906f}, (f32x2){0.6931471805599453f, 0.6931471805599453f});
}

__global__ __launch_bounds__(768) void fused_block(const uint16_t* __restrict__ x, const uint16_t* __restrict__ wqkv,
                                                   const uint16_t* __restrict__ wo, const uint16_t* __restrict__ w1,
                                                   const uint16_t* __restrict__ b1, const uint16_t* __restrict__ w2,
                                                   uint16_t* __restrict__ out, char* ws) {
  __shared__ __attribute__((aligned(1024))) char smem[LDS_BYTES];
  asm volatile("" ::"v"((AS3 char*)smem) : "memory");
  const int tid = threadIdx.x, lane = tid & 63;
  const int wid = rfl(tid >> 6);
  const int xcd = blockIdx.x & 7, jb = blockIdx.x >> 3;
  const int rb = (xcd & 1) * 8 + (jb & 7), cb = (xcd >> 1) * 4 + (jb >> 3);
  const int head = xcd * 4 + (jb & 3), qb = jb >> 2;
  const int om = xcd * 4 + (jb & 3), on = jb >> 2;
  const rsrc_t rws = __builtin_amdgcn_make_buffer_rsrc(ws, 0, WS_END, 0x00020000);
  const unsigned lane16 = lane * 16;
  asm volatile("" ::"s"(x), "s"(wqkv), "s"(wo), "s"(w1), "s"(b1), "s"(w2), "s"(out), "s"(ws));
  STAMP(0);

  if (wid >= 8) {
    const int g = wid - 8;
    const int psel = lane >> 4;
    const unsigned pflag = WS_QKFLAG + (((psel == 0) ? 8 + (head >> 2) : (psel == 1) ? (head >> 1) : (head >> 2)) * 16 +
                                        ((psel < 2) ? (lane & 15) : 2 * qb + (lane & 1))) * 8;
    u64 tok;
    {
      const rsrc_t rx = __builtin_amdgcn_make_buffer_rsrc((void*)x, 0, 2048 * 512 * 2, 0x00020000);
      const unsigned vx0 = (lane >> 3) * 1024 + (((lane & 7) ^ (lane >> 4)) << 4), vx1 = vx0 ^ 64;
      const unsigned vwc = ((lane & 7) ^ (((lane >> 4) & 1) << 2)) << 4;
      const int un = g >> 1, wgi = (cb - 14) * 16 + rb;
#pragma nounroll
      for (int i = 0; i < 16; ++i) {
        if (i >= 2 && i != 3) {
          if (i == 2) WAITV(8); else WAITV(16);
          BAR_RAW();
          LSTAMP(i == 2 ? 0 : i - 3);
        }
        const int s = i & 7;
        const unsigned slot = (unsigned)(i & 3) * SLOT;
        const void* wp; unsigned wbytes, stride, wsoff;
        if (i < 8) { wp = wqkv; wbytes = 512u * 3072u; stride = 3072u; wsoff = (un ? 2048u + 128u * (cb >> 1) : 128u * cb) + 64u * s * 3072u; }
        else if (cb < 14) { wp = w1; wbytes = 512u * 3584u; stride = 3584u; wsoff = 256u * cb + 128u * un + 64u * s * 3584u; }
        else {
          const int u = min(wgi + 32 * max(s - 3, 0), 143);
          wp = (u < 112) ? (const void*)w2 : (const void*)wo;
          wbytes = (u < 112) ? 1792u * 1024u : 512u * 1024u; stride = 1024u;
          wsoff = (unsigned)(((u < 112) ? u : u - 112) >> 2) * 65536u + (unsigned)(u & 3) * 256u + 128u * un;
        }
        const rsrc_t rw = __builtin_amdgcn_make_buffer_rsrc((void*)wp, 0, wbytes, 0x00020000);
        const unsigned vw = (lane >> 3) * stride + vwc;
        wsoff += 32u * (g & 1) * stride;
#pragma unroll
        for (int p = 0; p < 4; ++p) {
          dma(rx, (p & 1) ? vx1 : vx0, (unsigned)(128 * rb + 32 * g + 8 * p) * 1024u + 128u * s, slot + (32 * g + 8 * p) * 128);
          dma(rw, vw, wsoff + 8u * p * stride, slot + 16384 + g * 4096 + p * 1024);
        }
      }
      { const unsigned one = (lane & 11) ? 0u : 0x3f803f80u;
        lds_st<u32x4>(L_ONES + g * 1024 + lane16, (u32x4){one, one, one, one}); }
      tok = lds_tok();
      WAITV(16); BAR_RAW();
      LSTAMP(13);
      WAITV(8); BAR_RAW();
      LSTAMP(14);
      WAITV(0); BAR_RAW();
      LSTAMP(15);
      if (g == 0) {
        const u64 a = __hip_atomic_load((gu64*)(ws + pflag), RLX_AGENT);
        if (__all(a == tok)) lds_st<unsigned>(M_GO, 1u);
      }
      BAR();
      LSTAMP(16); LFLUSH();
    }
    STAMP(2);
    if (!rfl(*(volatile AS3 unsigned*)(uintptr_t)M_GO)) {
      if (g == 0) {
        poll1(ws, pflag, tok);
        lds_st<unsigned>(M_GO, 1u);
      } else {
#pragma nounroll
        for (unsigned spins = 0; spins < 64 * SPIN_CAP; ++spins) {
          if (rfl(*(volatile AS3 unsigned*)(uintptr_t)M_GO)) break;
          __builtin_amdgcn_s_sleep(1);
        }
      }
    }
    asm volatile("" ::: "memory");
    STAMP(3);
    {
      const unsigned hq = WS_Q + head * 65536u + qb * 8192u, hk = WS_K + head * 65536u, hv = WS_V + head * 65536u;
      dma_sc1(rws, lane16, hq + (2 * g) * 1024, L_Q + (2 * g) * 1024);
      dma_sc1(rws, lane16, hq + (2 * g + 1) * 1024, L_Q + (2 * g + 1) * 1024);
#pragma unroll
      for (int i = 0; i < 16; ++i) {
        const unsigned t = (unsigned)(4 * i + g) * 1024u;
        dma_sc1(rws, lane16, hk + t, L_K + t);
        dma_sc1(rws, lane16, hv + t, L_V + t);
      }
    }
    WAITV(26); BAR();
    STAMP(4);
    WAITV(0); BAR_RAW();
    STAMP(5);
    __builtin_amdgcn_s_setprio(3);
    {
      const int wg = (on >> 1) + 4 * (lane & 7);
      poll1(ws, WS_ACTFLAG + ((lane < 32) ? (om >> 1) * 16 + min(lane, 13) : (wg & 15) * 16 + 14 + (wg >> 4)) * 8, tok);
    }
    STAMP(6);
    f32x16 acc[2][2] = {};
    u32x4 wof[8][2];
    {
      const unsigned sa0 = WS_ACT + ((2 * om) * 112 + 28 * g) * 1024u, sa1 = sa0 + 112 * 1024u;
      const unsigned sw = WS_W2F + (28 * g * 16 + 2 * on) * 1024u;
      const unsigned so = WS_W2F + ((112 + 8 * g) * 16 + 2 * on) * 1024u;
      u32x4 fa[4][2], fw[4][2];
#pragma unroll
      for (int d = 0; d < 4; ++d) {
        fa[d][0] = ld_sc1(rws, lane16, sa0 + d * 1024); fa[d][1] = ld_sc1(rws, lane16, sa1 + d * 1024);
        fw[d][0] = ld_sc1(rws, lane16, sw + d * 16384); fw[d][1] = ld_sc1(rws, lane16 + 1024, sw + d * 16384);
      }
      SBAR();
#pragma unroll
      for (int k = 0; k < 28; ++k) {
        const int d = k & 3;
#pragma unroll
        for (int i = 0; i < 2; ++i)
#pragma unroll
          for (int j = 0; j < 2; ++j)
            acc[i][j] = MFMA32(__builtin_bit_cast(bf16x8, fw[d][j]), __builtin_bit_cast(bf16x8, fa[d][i]), acc[i][j]);
        SBAR();
        if (k + 4 < 28) {
          fa[d][0] = ld_sc1(rws, lane16, sa0 + (k + 4) * 1024); fa[d][1] = ld_sc1(rws, lane16, sa1 + (k + 4) * 1024);
          fw[d][0] = ld_sc1(rws, lane16, sw + (k + 4) * 16384); fw[d][1] = ld_sc1(rws, lane16 + 1024, sw + (k + 4) * 16384);
        } else {
          wof[2 * d][0] = ld_sc1(rws, lane16, so + (2 * d) * 16384); wof[2 * d][1] = ld_sc1(rws, lane16 + 1024, so + (2 * d) * 16384);
          wof[2 * d + 1][0] = ld_sc1(rws, lane16, so + (2 * d + 1) * 16384); wof[2 * d + 1][1] = ld_sc1(rws, lane16 + 1024, so + (2 * d + 1) * 16384);
        }
        SBAR();
      }
    }
    STAMP(7);
    BAR_RAW();
    STAMP(8);
    poll1(ws, WS_OFLAG + ((2 * om + ((lane >> 3) & 1)) * 32 + 8 * g + (lane & 7)) * 8, tok);
    STAMP(9);
    {
      const unsigned so = WS_O + ((2 * om) * 32 + 8 * g) * 1024u, lo = g * 16384;
#pragma unroll
      for (int k = 0; k < 8; ++k) {
        dma_sc1(rws, lane16, so + k * 1024, lo + k * 2048);
        dma_sc1(rws, lane16, so + 32768 + k * 1024, lo + k * 2048 + 1024);
      }
#define WOSTEP(k, n) do { WAITV(n); \
        const bf16x8 o0 = lds_ld<bf16x8>(lo + (k) * 2048 + lane16), o1 = lds_ld<bf16x8>(lo + (k) * 2048 + 1024 + lane16); \
        acc[0][0] = MFMA32(__builtin_bit_cast(bf16x8, wof[k][0]), o0, acc[0][0]); acc[0][1] = MFMA32(__builtin_bit_cast(bf16x8, wof[k][1]), o0, acc[0][1]); \
        acc[1][0] = MFMA32(__builtin_bit_cast(bf16x8, wof[k][0]), o1, acc[1][0]); acc[1][1] = MFMA32(__builtin_bit_cast(bf16x8, wof[k][1]), o1, acc[1][1]); } while (0)
      WOSTEP(0, 14); WOSTEP(1, 12); WOSTEP(2, 10); WOSTEP(3, 8); WOSTEP(4, 6); WOSTEP(5, 4); WOSTEP(6, 2); WOSTEP(7, 0);
#undef WOSTEP
    }
    {
      const unsigned pb = L_PART + g * PART_SZ + (lane & 31) * PART_ROW + (lane >> 5) * 16;
#pragma unroll
      for (int i = 0; i < 2; ++i)
#pragma unroll
        for (int j = 0; j < 2; ++j)
#pragma unroll
          for (int q = 0; q < 4; ++q)
            lds_st<f32x4>(pb + i * 32 * PART_ROW + j * 128 + q * 32, (f32x4){acc[i][j][4 * q], acc[i][j][4 * q + 1], acc[i][j][4 * q + 2], acc[i][j][4 * q + 3]});
    }
    STAMP(10);
    BAR();
    return;
  }

  const int r = lane & 31, h = lane >> 5;
  u64 tok;
  u32x4 actp[4];
  const float LOG2E = 1.4426950408889634f;
  {
    if (wid == 0) {
      const u64 e = __hip_atomic_load((gu64*)(ws + WS_EPOCH), RLX_AGENT) + 1;
      if (lane == 0) lds_st<u32x2>(M_TOK, (u32x2){(unsigned)e, (unsigned)(e >> 32)});
    } else if (wid == 1) {
      lds_st<u32x4>(L_MISC + lane16, (u32x4){0, 0, 0, 0});
      if (lane == 0) lds_st<u32x4>(M_CNT, (u32x4){0, 0, 0, 0});
    } else if (wid < 4 && cb < 14) {
      const int c = (wid - 2) * 64 + lane;
      lds_st<float>(M_B1 + c * 4, bflo(b1[128 * cb + c]) * LOG2E);
    }
    const int w = wid & 3, tt = rb * 4 + w;
    f32x16 a0 = {}, a1 = {};
    f32x4 cs = {};
    float sx = 0.f, sxx = 0.f;
    PIN(a0); PIN(a1); PIN(cs); SBAR();
    BAR();
    if (wid < 4) {
      gemm_pass<0>(lane, w * 4096, 16384, w & 1, w >> 1, a0, a1, cs, sx, sxx);
      STAMP(1);
      cs_add(M_CSQK + (w & 1) * 128, cs, lane);
      BAR();
      tok = lds_tok();
      float rs = lds_ld<float>(M_RS + (32 * w + r) * 4), mr = lds_ld<float>(M_MR + (32 * w + r) * 4);
      if (cb < 8) { rs *= 0.25f * LOG2E; mr *= 0.25f * LOG2E; }
      const unsigned base = ((cb < 8) ? WS_Q : WS_K) + ((4 * (cb & 7)) * 64 + tt) * 1024u;
#pragma unroll
      for (int t = 0; t < 2; ++t) {
        const int ct = (w & 1) ^ t;
        const f32x16 a = t ? a1 : a0;
        float c[16];
        ld_rows(c, M_CSQK + 128 * ct, h);
        u32x4 p0, p1;
#pragma unroll
        for (int i = 0; i < 4; ++i) {
          p0[i] = pk2(a[2 * i] * rs - mr * c[2 * i], a[2 * i + 1] * rs - mr * c[2 * i + 1]);
          p1[i] = pk2(a[8 + 2 * i] * rs - mr * c[8 + 2 * i], a[9 + 2 * i] * rs - mr * c[9 + 2 * i]);
        }
        st_sc1(p0, rws, lane16, base + (2 * ct) * 65536u); st_sc1(p1, rws, lane16, base + (2 * ct + 1) * 65536u);
      }
    } else {
      gemm_pass<1>(lane, w * 4096, 16384 + 8192, cb & 1, w & 1, a0, a1, cs, sx, sxx);
      STAMP(1);
      sx = half_sum(sx); sxx = half_sum(sxx);
      const float mu = sx * (1.0f / 512.0f);
      const float var = fmaxf(sxx * (1.0f / 512.0f) - mu * mu, 0.f);
      const float rs = __builtin_amdgcn_rsqf(var + 1e-5f);
      if (lane < 32) { lds_st<float>(M_RS + (32 * w + r) * 4, rs); lds_st<float>(M_MR + (32 * w + r) * 4, mu * rs); }
      cs_add(M_CSV + (w >> 1) * 128, cs, lane);
      BAR();
      tok = lds_tok();
      float t0[16], t1[16];
      ld_rows(t0, M_RS + 128 * w, h); ld_rows(t1, M_MR + 128 * w, h);
      const float csv = lds_ld<float>(M_CSV + (w >> 1) * 128 + r * 4);
      u32x4 p0, p1;
#pragma unroll
      for (int i = 0; i < 4; ++i) {
        p0[i] = pk2(a0[2 * i] * t0[2 * i] - t1[2 * i] * csv, a0[2 * i + 1] * t0[2 * i + 1] - t1[2 * i + 1] * csv);
        p1[i] = pk2(a0[8 + 2 * i] * t0[8 + 2 * i] - t1[8 + 2 * i] * csv, a0[9 + 2 * i] * t0[9 + 2 * i] - t1[9 + 2 * i] * csv);
      }
      const unsigned vo = (r >> 4) * 65536 + h * 256 + (r & 15) * 16, so = WS_V + (2 * cb * 64 + tt) * 1024u;
      st_sc1(p0, rws, vo, so); st_sc1(p1, rws, vo + 512, so);
    }
    STAMP(2);
  }
  if (cb < 14) {
    const int rp = wid & 1, ct = wid >> 1;
    f32x16 a0 = {}, a1 = {};
    f32x4 cs = {};
    float sx, sxx;
    gemm_pass<2>(lane, rp * 8192, 16384 + (ct >> 1) * 8192, ct & 1, rp, a0, a1, cs, sx, sxx, ws, WS_QKFLAG + (cb * 16 + rb) * 8, tok);
    cs_add(M_CSF + ct * 128, cs, lane);
    BAR();
    float c[16], bb[16];
    ld_rows(c, M_CSF + ct * 128, h); ld_rows(bb, M_B1 + ct * 128, h);
#pragma unroll
    for (int i = 0; i < 2; ++i) {
      const float rs = lds_ld<float>(M_RS + (64 * rp + 32 * i + r) * 4) * LOG2E, mr = lds_ld<float>(M_MR + (64 * rp + 32 * i + r) * 4) * -LOG2E;
      const f32x16 a = i ? a1 : a0;
      unsigned p[8];
#pragma unroll
      for (int e = 0; e < 8; ++e) {
        const f32x2 off = __builtin_elementwise_fma((f32x2){c[2 * e], c[2 * e + 1]}, (f32x2){mr, mr}, (f32x2){bb[2 * e], bb[2 * e + 1]});
        const f32x2 m = mish2(__builtin_elementwise_fma((f32x2){a[2 * e], a[2 * e + 1]}, (f32x2){rs, rs}, off));
        p[e] = pk2(m[0], m[1]);
      }
      actp[2 * i] = (u32x4){p[0], p[1], p[2], p[3]}; actp[2 * i + 1] = (u32x4){p[4], p[5], p[6], p[7]};
    }
  } else {
    publish_wg(ws, WS_QKFLAG + (cb * 16 + rb) * 8, M_CNT, tok, lane);
    const int wgi = (cb - 14) * 16 + rb, q4 = (lane & 15) >> 2, ks = wid & 3, un = wid >> 2;
    const unsigned wl = 16384 + un * 8192 + ks * 2048 + (4 * h + q4) * 128 + ((lane >> 4) & 1) * 32 + (lane & 3) * 8 + ((q4 >> 1) << 6);
#pragma nounroll
    for (int i = 0; i < 8; ++i) {
      const int u = wgi + 32 * (i - 3);
      if (i >= 3 && u < 144) {
        const unsigned a = wl + (unsigned)(i & 3) * SLOT;
#pragma unroll
        for (int ct = 0; ct < 2; ++ct) {
          const unsigned aa = a ^ (ct << 6);
          const bf16x8 f = lds_tr2(aa, aa + 1024);
          st_sc1(__builtin_bit_cast(u32x4, f), rws, lane16, WS_W2F + ((4 * (u >> 2) + ks) * 16 + 4 * (u & 3) + 2 * un + ct) * 1024u);
        }
      }
      BAR();
    }
  }
  STAMP(3);

  f32x16 o = {};
  bf16x8 v0, v1 = {};
  u32x4 p0, p1 = {0, 0, 0, 0};
  PIN(o); PIN(v1); PIN(p1); SBAR();
  BAR_RAW();
  STAMP(4);
  if (cb < 14) {
#pragma unroll
    for (int i = 0; i < 2; ++i) {
      const unsigned so = WS_ACT + ((rb * 4 + 2 * (wid & 1) + i) * 112 + 8 * cb + 2 * (wid >> 1)) * 1024u;
      st_sc1(actp[2 * i], rws, lane16, so); st_sc1(actp[2 * i + 1], rws, lane16 + 1024, so);
    }
  }
  {
    const bf16x8 q = lds_ld<bf16x8>(L_Q + wid * 1024 + lane16);
    unsigned kp = L_K + lane16;
    const bool real = (lane & 16) == 0;
    const unsigned vp0 = (real ? L_V : L_ONES) + h * 256 + (lane & 15) * 16;
    unsigned vp = vp0;
    const unsigned vinc = real ? 4096u : 0u;
    const f32x16 zero = {};
    bf16x8 kn = lds_ld<bf16x8>(kp);
    f32x16 sA = MFMA32(kn, q, zero), sB;
    kn = lds_ld<bf16x8>(kp + 1024);
    float m = fmaxf(sA[0], sA[1]);
#pragma unroll
    for (int i = 2; i < 16; i += 2) m = fmaxf(fmaxf(m, sA[i]), sA[i + 1]);
    m = half_max(m);
    f32x16 negm;
#pragma unroll
    for (int i = 0; i < 16; ++i) { negm[i] = -m; sA[i] -= m; }
    PIN(negm);
    __builtin_amdgcn_s_setprio(2);
#define E2(S, i) do { S[i] = EX2(S[i]); S[i + 1] = EX2(S[i + 1]); } while (0)
#define STEP(SC, SN, u) do { \
    E2(SC, 0); PIN(SC); SBAR(); \
    o = MFMA32(v1, __builtin_bit_cast(bf16x8, p1), o); PIN(o); SBAR(); \
    v0 = lds_ld<bf16x8>(vp + (u) * 1024); v1 = lds_ld<bf16x8>(vp + (u) * 1024 + 512); SBAR(); \
    E2(SC, 2); E2(SC, 4); PIN(SC); SBAR(); \
    SN = MFMA32(kn, q, negm); PIN(SN); SBAR(); \
    kn = lds_ld<bf16x8>(kp + ((u) + 2) * 1024); SBAR(); \
    E2(SC, 6); E2(SC, 8); \
    p0[0] = pk2(SC[0], SC[1]); p0[1] = pk2(SC[2], SC[3]); p0[2] = pk2(SC[4], SC[5]); p0[3] = pk2(SC[6], SC[7]); PIN(p0); PIN(SC); SBAR(); \
    E2(SC, 10); PIN(SC); SBAR(); \
    o = MFMA32(v0, __builtin_bit_cast(bf16x8, p0), o); PIN(o); SBAR(); \
    E2(SC, 12); E2(SC, 14); \
    p1[0] = pk2(SC[8], SC[9]); p1[1] = pk2(SC[10], SC[11]); p1[2] = pk2(SC[12], SC[13]); p1[3] = pk2(SC[14], SC[15]); PIN(p1); SBAR(); \
  } while (0)
#pragma nounroll
    for (int it = 0; it < 16; ++it) {
      PIN(kp); PIN(vp);
      STEP(sA, sB, 0); STEP(sB, sA, 1); STEP(sA, sB, 2); STEP(sB, sA, 3);
      kp += 4096; vp += vinc;
      if ((0x480a >> it) & 1) {
        if (it == 1) BAR_RAW();
        else if (it == 3) publish_wg(ws, WS_ACTFLAG + (rb * 16 + cb) * 8, M_CNT2, tok, lane);
        else if (it == 11) __builtin_amdgcn_s_setprio(1);
        else __builtin_amdgcn_s_setprio(0);
      }
    }
#undef STEP
#undef E2
    o = MFMA32(v1, __builtin_bit_cast(bf16x8, p1), o);
    STAMP(5);
    float chk = o[8];
#pragma unroll
    for (int i = 0; i < 8; ++i) chk += fabsf(o[i]) * 0x1p-40f;
    if (__any(!(chk < 0x1p64f))) {
      float mm = -3.0e38f;
      o = zero;
#pragma nounroll
      for (int t = 0; t < 64; ++t) {
        f32x16 s = MFMA32(lds_ld<bf16x8>(L_K + lane16 + t * 1024), q, zero);
        float tm = s[0];
#pragma unroll
        for (int i = 1; i < 16; ++i) tm = fmaxf(tm, s[i]);
        const float mn = fmaxf(mm, half_max(tm));
        const float al = EX2(mm - mn);
        mm = mn;
        u32x4 pa, pb;
#pragma unroll
        for (int i = 0; i < 4; ++i) {
          pa[i] = pk2(EX2(s[2 * i] - mn), EX2(s[2 * i + 1] - mn));
          pb[i] = pk2(EX2(s[8 + 2 * i] - mn), EX2(s[9 + 2 * i] - mn));
        }
#pragma unroll
        for (int i = 0; i < 16; ++i) o[i] *= al;
        const unsigned va = vp0 + (real ? t * 1024 : 0);
        o = MFMA32(lds_ld<bf16x8>(va), __builtin_bit_cast(bf16x8, pa), o);
        o = MFMA32(lds_ld<bf16x8>(va + 512), __builtin_bit_cast(bf16x8, pb), o);
      }
    }
    const float rl = __builtin_amdgcn_rcpf(o[8]);
    const u32x4 ov = {pk2(o[0] * rl, o[1] * rl), pk2(o[2] * rl, o[3] * rl), pk2(o[4] * rl, o[5] * rl), pk2(o[6] * rl, o[7] * rl)};
    const int qt = qb * 8 + wid;
    st_sc1(ov, rws, lane16, WS_O + (qt * 32 + head) * 1024u);
    publish(ws, WS_OFLAG + (qt * 32 + head) * 8, tok, lane);
    STAMP(6);
    BAR();
  }
  {
    const int row = tid >> 3, c8 = tid & 7;
    const size_t gi = (size_t)(64 * om + row) * 512 + 64 * on + 8 * c8;
    const u32x4 xv = *(const u32x4*)(x + gi);
    BAR_RAW();
    STAMP(7);
    f32x4 s0 = {bflo(xv[0]), bfhi(xv[0]), bflo(xv[1]), bfhi(xv[1])}, s1 = {bflo(xv[2]), bfhi(xv[2]), bflo(xv[3]), bfhi(xv[3])};
    const unsigned pa = L_PART + row * PART_ROW + c8 * 32;
#pragma unroll
    for (int g = 0; g < 4; ++g) { s0 += lds_ld<f32x4>(pa + g * PART_SZ); s1 += lds_ld<f32x4>(pa + g * PART_SZ + 16); }
    *(u32x4*)(out + gi) = (u32x4){pk2(s0[0], s0[1]), pk2(s0[2], s0[3]), pk2(s1[0], s1[1]), pk2(s1[2], s1[3])};
    if (blockIdx.x == 0 && tid == 0) __hip_atomic_store((gu64*)(ws + WS_EPOCH), tok, RLX_AGENT);
    STAMP(8);
  }
}

extern "C" void kernel_launch(void* const* d_in, const int* in_sizes, int n_in, void* d_out, int out_size, void* d_ws,
                              size_t ws_size, hipStream_t stream) {
  fused_block<<<256, 768, 0, stream>>>((const uint16_t*)d_in[0], (const uint16_t*)d_in[1], (const uint16_t*)d_in[2],
                                       (const uint16_t*)d_in[4], (const uint16_t*)d_in[5], (const uint16_t*)d_in[6],
                                       (uint16_t*)d_out, (char*)d_ws);
}
```

```cpp
#include <hip/hip_runtime.h>
#include <stdint.h>

typedef __attribute__((ext_vector_type(8))) __bf16 bf16x8;
typedef __attribute__((ext_vector_type(2))) __bf16 bf16x2;
typedef __attribute__((ext_vector_type(2))) float f32x2;
typedef __attribute__((ext_vector_type(4))) float f32x4;
typedef __attribute__((ext_vector_type(16))) float f32x16;
typedef __attribute__((ext_vector_type(2))) unsigned u32x2;
typedef __attribute__((ext_vector_type(4))) unsigned u32x4;
typedef __attribute__((ext_vector_type(4))) short s16x4;
typedef unsigned long long u64;
typedef __attribute__((address_space(1))) u64 gu64;
typedef __amdgpu_buffer_rsrc_t rsrc_t;
#define AS3 __attribute__((address_space(3)))
#define DI __device__ __forceinline__

constexpr unsigned WS_EPOCH = 0;
constexpr unsigned WS_QKFLAG = 4096;
constexpr unsigned WS_ACTFLAG = WS_QKFLAG + 8192;
constexpr unsigned WS_OFLAG = WS_ACTFLAG + 8192;
constexpr unsigned WS_STAMP = 65536;
constexpr unsigned WS_Q = 1u << 20;
constexpr unsigned WS_K = WS_Q + (2u << 20);
constexpr unsigned WS_V = WS_K + (2u << 20);
constexpr unsigned WS_O = WS_V + (2u << 20);
constexpr unsigned WS_ACT = WS_O + (2u << 20);
constexpr unsigned WS_W2F = WS_ACT + 64u * 112u * 1024u;
constexpr unsigned WS_END = WS_W2F + 144u * 16u * 1024u;

constexpr unsigned SLOT = 49152;
constexpr unsigned L_K = 0, L_V = 65536, L_ONES = 131072, L_Q = 135168;
constexpr unsigned L_PART = 65536, PART_ROW = 272, PART_SZ = 64 * PART_ROW;
constexpr unsigned L_MISC = 159744;
constexpr unsigned M_RS = L_MISC, M_MR = M_RS + 512, M_CSQK = M_MR + 512, M_CSV = M_CSQK + 256, M_CSF = M_CSV + 128,
                   M_B1 = M_CSF + 512, M_TOK = M_B1 + 512, M_CNT = M_TOK + 16, M_GO = M_CNT + 16;
constexpr unsigned LDS_BYTES = 163840;

constexpr unsigned SPIN_CAP = 40000;

#define MFMA32(a, b, c) __builtin_amdgcn_mfma_f32_32x32x16_bf16(a, b, c, 0, 0, 0)
#define MFMA16(a, b, c) __builtin_amdgcn_mfma_f32_16x16x32_bf16(a, b, c, 0, 0, 0)
#define SBAR() __builtin_amdgcn_sched_barrier(0)
#define PIN(x) asm volatile("" : "+v"(x))
#define BAR() asm volatile("s_waitcnt lgkmcnt(0)\n\ts_barrier" ::: "memory")
#define WAITL0() __builtin_amdgcn_s_waitcnt(0xC07F)
#define BAR_RAW() asm volatile("s_barrier" ::: "memory")
#define WAITV(n) asm volatile("s_waitcnt vmcnt(" #n ")" ::: "memory")
#define RLX_AGENT __ATOMIC_RELAXED, __HIP_MEMORY_SCOPE_AGENT
#define EX2(x) __builtin_amdgcn_exp2f(x)

#define STAMP(i) do { } while (0)

template <typename T> DI T lds_ld(unsigned a) { return *(const AS3 T*)(uintptr_t)a; }
template <typename T> DI void lds_st(unsigned a, T v) { *(AS3 T*)(uintptr_t)a = v; }
DI bf16x8 lds_tr2(unsigned a0, unsigned a1) {
  s16x4 lo = __builtin_amdgcn_ds_read_tr16_b64_v4i16((AS3 s16x4*)(uintptr_t)a0);
  s16x4 hi = __builtin_amdgcn_ds_read_tr16_b64_v4i16((AS3 s16x4*)(uintptr_t)a1);
  return __builtin_bit_cast(bf16x8, __builtin_shufflevector(lo, hi, 0, 1, 2, 3, 4, 5, 6, 7));
}
DI unsigned pk2(float a, float b) { f32x2 v = {a, b}; return __builtin_bit_cast(unsigned, __builtin_convertvector(v, bf16x2)); }
DI float bflo(unsigned u) { return __uint_as_float(u << 16); }
DI float bfhi(unsigned u) { return __uint_as_float(u & 0xffff0000u); }
DI float half_sum(float v) { auto r = __builtin_amdgcn_permlane32_swap(__float_as_uint(v), __float_as_uint(v), false, false); return __uint_as_float(r[0]) + __uint_as_float(r[1]); }
DI float half_max(float v) { auto r = __builtin_amdgcn_permlane32_swap(__float_as_uint(v), __float_as_uint(v), false, false); return fmaxf(__uint_as_float(r[0]), __uint_as_float(r[1])); }
DI unsigned rfl(unsigned v) { return __builtin_amdgcn_readfirstlane(v); }
DI void dma(rsrc_t r, unsigned voff, unsigned soff, unsigned ldsdst) {
  asm volatile("s_mov_b32 m0, %0\n\ts_nop 0\n\tbuffer_load_dwordx4 %1, %2, %3 offen lds" ::"s"(ldsdst), "v"(voff), "s"(r), "s"(soff) : "memory");
}
DI void dma_sc1(rsrc_t r, unsigned voff, unsigned soff, unsigned ldsdst) {
  asm volatile("s_mov_b32 m0, %0\n\ts_nop 0\n\tbuffer_load_dwordx4 %1, %2, %3 offen sc1 lds" ::"s"(ldsdst), "v"(voff), "s"(r), "s"(soff) : "memory");
}
DI void st_sc1(u32x4 v, rsrc_t r, unsigned voff, unsigned soff) { __builtin_amdgcn_raw_buffer_store_b128(v, r, voff, soff, 16); }
DI u32x4 ld_sc1(rsrc_t r, unsigned voff, unsigned soff) { return __builtin_amdgcn_raw_buffer_load_b128(r, voff, soff, 16); }
DI void publish(char* ws, unsigned off, u64 tok, int lane) {
  WAITV(0);
  if (lane == 0) __hip_atomic_store((gu64*)(ws + off), tok, RLX_AGENT);
}
DI void poll1(char* ws, unsigned off, u64 tok) {
#pragma nounroll
  for (unsigned spins = 0; spins < SPIN_CAP; ++spins) {
    const u64 a = __hip_atomic_load((gu64*)(ws + off), RLX_AGENT);
    if (__all(a == tok)) break;
    __builtin_amdgcn_s_sleep(2);
  }
  asm volatile("" ::: "memory");
}
DI u64 lds_tok() { const u32x2 t = lds_ld<u32x2>(M_TOK); return ((u64)rfl(t[1]) << 32) | rfl(t[0]); }

DI void ld_rows(float (&d)[16], unsigned base, int h) {
#pragma unroll
  for (int g = 0; g < 4; ++g) {
    const f32x4 v = lds_ld<f32x4>(base + 16 * h + 32 * g);
    d[4 * g] = v[0]; d[4 * g + 1] = v[1]; d[4 * g + 2] = v[2]; d[4 * g + 3] = v[3];
  }
}

template <bool SUMV>
DI void qkv_mainloop(int lane, int w, int cb, f32x16& a0, f32x16& a1, f32x16& a2, float& sx, float& sxx, f32x4& cs) {
  const int r = lane & 31, h = lane >> 5, q4 = (lane & 15) >> 2;
  unsigned xa[4];
#pragma unroll
  for (int ks = 0; ks < 4; ++ks) xa[ks] = w * 4096 + r * 128 + ((((h ^ ((r >> 1) & 7))) ^ (2 * ks)) << 4);
  const unsigned wl = 16384 + (8 * h + q4) * 128 + ((lane >> 4) & 1) * 32 + (lane & 3) * 8;
  unsigned wa0 = wl + (((w & 1) ^ (q4 >> 1)) << 6), wa1 = wa0 ^ 64, wav = wl + 8192 + (((cb & 1) ^ (q4 >> 1)) << 6);
  const unsigned one2 = ((lane & 15) == ((lane >> 4) & 1)) ? 0x3f803f80u : 0u;
  const bf16x8 sel = __builtin_bit_cast(bf16x8, (u32x4){one2, one2, one2, one2});
  bf16x8 xA, w0A, w1A, wvA, xB, w0B, w1B, wvB;
  float sx1 = 0.f, sxx1 = 0.f;
#define QRX(X, ks) X = lds_ld<bf16x8>(xa[ks])
#define QRW(W, A, ks) W = lds_tr2(A + (ks) * 2048, A + (ks) * 2048 + 512)
#define QST(X, i) do { const unsigned u_ = __builtin_bit_cast(u32x4, X)[i]; const float l_ = bflo(u_), h_ = bfhi(u_); \
    sx += l_; sx1 += h_; sxx = __builtin_fmaf(l_, l_, sxx); sxx1 = __builtin_fmaf(h_, h_, sxx1); PIN(sx); PIN(sx1); PIN(sxx); PIN(sxx1); } while (0)
#define QSTEP(X, W0, W1, WV, Y, U0, U1, UV, ks) do { \
    a0 = MFMA32(W0, X, a0); SBAR(); QRX(Y, ks); QRW(U0, wa0, ks); QST(X, 0); SBAR(); \
    a1 = MFMA32(W1, X, a1); SBAR(); QRW(U1, wa1, ks); QST(X, 1); SBAR(); \
    a2 = MFMA32(X, WV, a2); SBAR(); QRW(UV, wav, ks); QST(X, 2); SBAR(); \
    cs = MFMA16(sel, SUMV ? WV : W0, cs); QST(X, 3); SBAR(); } while (0)
  PIN(a0); PIN(a1); PIN(a2); PIN(cs); SBAR();
  BAR();
  QRX(xA, 0); QRW(w0A, wa0, 0); QRW(w1A, wa1, 0); QRW(wvA, wav, 0); SBAR();
  int slot = 0;
#pragma nounroll
  for (int s = 0; s < 8; ++s) {
    QSTEP(xA, w0A, w1A, wvA, xB, w0B, w1B, wvB, 1);
    QSTEP(xB, w0B, w1B, wvB, xA, w0A, w1A, wvA, 2);
    QSTEP(xA, w0A, w1A, wvA, xB, w0B, w1B, wvB, 3);
    WAITL0(); SBAR();
    if (s < 7) {
      const int d = (slot == 2) ? -2 * (int)SLOT : (int)SLOT;
      slot = (slot == 2) ? 0 : slot + 1;
      xa[0] += d; xa[1] += d; xa[2] += d; xa[3] += d; wa0 += d; wa1 += d; wav += d;
      BAR_RAW();
    }
    QSTEP(xB, w0B, w1B, wvB, xA, w0A, w1A, wvA, 0);
  }
  sx += sx1; sxx += sxx1;
#undef QRX
#undef QRW
#undef QST
#undef QSTEP
}

DI f32x2 mish2(f32x2 h) {
  const f32x2 z = h * 1.4426950408889634f;
  f32x2 t; t[0] = EX2(z[0]); t[1] = EX2(z[1]);
  const f32x2 d = __builtin_elementwise_fma(t, t + 2.0f, (f32x2){2.0f, 2.0f});
  f32x2 rc; rc[0] = __builtin_amdgcn_rcpf(d[0]); rc[1] = __builtin_amdgcn_rcpf(d[1]);
  return h * __builtin_elementwise_fma(rc, (f32x2){-2.0f, -2.0f}, (f32x2){1.0f, 1.0f});
}

__global__ __launch_bounds__(768) void fused_block(const uint16_t* __restrict__ x, const uint16_t* __restrict__ wqkv,
                                                   const uint16_t* __restrict__ wo, const uint16_t* __restrict__ w1,
                                                   const uint16_t* __restrict__ b1, const uint16_t* __restrict__ w2,
                                                   uint16_t* __restrict__ out, char* ws) {
  __shared__ __attribute__((aligned(1024))) char smem[LDS_BYTES];
  asm volatile("" ::"v"((AS3 char*)smem) : "memory");
  const int tid = threadIdx.x, lane = tid & 63;
  const int wid = rfl(tid >> 6);
  const int xcd = blockIdx.x & 7, jb = blockIdx.x >> 3;
  const int rb = (xcd & 1) * 8 + (jb & 7), cb = (xcd >> 1) * 4 + (jb >> 3);
  const int head = xcd * 4 + (jb & 3), qb = jb >> 2;
  const int om = xcd * 4 + (jb & 3), on = jb >> 2;
  const rsrc_t rws = __builtin_amdgcn_make_buffer_rsrc(ws, 0, WS_END, 0x00020000);
  const unsigned lane16 = lane * 16;
  asm volatile("" ::"s"(x), "s"(wqkv), "s"(wo), "s"(w1), "s"(b1), "s"(w2), "s"(out), "s"(ws));
  STAMP(0);

  if (wid >= 8) {
    const int g = wid - 8;
    {
      const rsrc_t rx = __builtin_amdgcn_make_buffer_rsrc((void*)x, 0, 2048 * 512 * 2, 0x00020000);
      const bool relay = (cb >= 14) && (g >= 2);
      const unsigned stride = (g < 2) ? 3072u : (relay ? 1024u : 3584u);
      const unsigned vx0 = (lane >> 3) * 1024 + (((lane & 7) ^ (lane >> 4)) << 4), vx1 = vx0 ^ 64;
      const unsigned vw = (lane >> 3) * stride + (((lane & 7) ^ (((lane >> 4) & 1) << 2)) << 4);
      const unsigned wcol = (g == 0) ? 128u * cb : (g == 1) ? 2048u + 128u * (cb >> 1) : 256u * cb + 128u * (g - 2);
      const int rot = 0;
      const int wgi = (cb - 14) * 16 + rb;
#pragma nounroll
      for (int i = 0; i < 8; ++i) {
        if (i >= 2) {
          WAITV(12); BAR_RAW();
          STAMP(i == 2 ? 1 : 8 + i);
        }
        const int s = (i + rot) & 7;
        const unsigned slot = (unsigned)(i % 3) * SLOT;
        const void* wp = (g < 2) ? (const void*)wqkv : (const void*)w1;
        unsigned wbytes = (g < 2) ? 512u * 3072u : 512u * 3584u, wsoff = wcol + 64u * s * stride;
        if (relay) {
          const int u = min(wgi + 32 * i, 143);
          wp = (u < 112) ? (const void*)w2 : (const void*)wo;
          wbytes = (u < 112) ? 1792u * 1024u : 512u * 1024u;
          wsoff = (unsigned)(((u < 112) ? u : u - 112) >> 2) * 65536u + (unsigned)(u & 3) * 256u + 128u * (g - 2);
        }
        const rsrc_t rw = __builtin_amdgcn_make_buffer_rsrc((void*)wp, 0, wbytes, 0x00020000);
#pragma unroll
        for (int p = 0; p < 4; ++p)
          dma(rx, (p & 1) ? vx1 : vx0, (unsigned)(128 * rb + 32 * g + 8 * p) * 1024u + 128u * s, slot + (32 * g + 8 * p) * 128);
#pragma unroll
        for (int p = 0; p < 8; ++p) dma(rw, vw, wsoff + 8u * p * stride, slot + 16384 + g * 8192 + p * 1024);
      }
      WAITV(12); BAR_RAW();
      WAITV(0); BAR_RAW();
      BAR_RAW();
    }
    STAMP(2);
    const u64 tok = lds_tok();
    { const unsigned one = (lane & 11) ? 0u : 0x3f803f80u;
      lds_st<u32x4>(L_ONES + g * 1024 + lane16, (u32x4){one, one, one, one}); }
    if (g == 0) {
      const int sel = lane >> 4;
      const int fcb = (sel == 0) ? 8 + (head >> 2) : (sel == 1) ? (head >> 1) : (head >> 2);
      const int frb = (sel < 2) ? (lane & 15) : 2 * qb + (lane & 1);
      poll1(ws, WS_QKFLAG + (fcb * 16 + frb) * 8, tok);
      lds_st<unsigned>(M_GO, 1u);
    } else {
#pragma nounroll
      for (unsigned spins = 0; spins < 64 * SPIN_CAP; ++spins) {
        if (rfl(*(volatile AS3 unsigned*)(uintptr_t)M_GO)) break;
        __builtin_amdgcn_s_sleep(1);
      }
      asm volatile("" ::: "memory");
    }
    STAMP(3);
    {
      const unsigned hq = WS_Q + head * 65536u + qb * 8192u, hk = WS_K + head * 65536u, hv = WS_V + head * 65536u;
      dma_sc1(rws, lane16, hq + (2 * g) * 1024, L_Q + (2 * g) * 1024);
      dma_sc1(rws, lane16, hq + (2 * g + 1) * 1024, L_Q + (2 * g + 1) * 1024);
#pragma unroll
      for (int i = 0; i < 16; ++i) {
        const unsigned t = (unsigned)(4 * i + g) * 1024u;
        dma_sc1(rws, lane16, hk + t, L_K + t);
        dma_sc1(rws, lane16, hv + t, L_V + t);
      }
    }
    WAITV(26); BAR();
    STAMP(4);
    WAITV(0); BAR_RAW();
    STAMP(5);
    __builtin_amdgcn_s_setprio(3);
    {
      const int arb = om >> 1, arp = om & 1;
      const unsigned fa = WS_ACTFLAG + ((arb * 2 + arp) * 32 + min(lane, 27)) * 8;
      const int li = lane & 15, wg = (on >> 1) + 4 * (li >> 1);
      const unsigned fr = WS_ACTFLAG + (((wg & 15) * 2 + (li & 1)) * 32 + (14 + (wg >> 4)) * 2 + (on & 1)) * 8;
      poll1(ws, (lane < 32) ? fa : fr, tok);
    }
    STAMP(6);
    f32x16 acc[2][2] = {};
    u32x4 wof[8][2];
    {
      const unsigned sa0 = WS_ACT + ((2 * om) * 112 + 28 * g) * 1024u, sa1 = sa0 + 112 * 1024u;
      const unsigned sw = WS_W2F + (28 * g * 16 + 2 * on) * 1024u;
      const unsigned so = WS_W2F + ((112 + 8 * g) * 16 + 2 * on) * 1024u;
      u32x4 fa[4][2], fw[4][2];
#pragma unroll
      for (int d = 0; d < 4; ++d) {
        fa[d][0] = ld_sc1(rws, lane16, sa0 + d * 1024); fa[d][1] = ld_sc1(rws, lane16, sa1 + d * 1024);
        fw[d][0] = ld_sc1(rws, lane16, sw + d * 16384); fw[d][1] = ld_sc1(rws, lane16 + 1024, sw + d * 16384);
      }
      SBAR();
#pragma unroll
      for (int k = 0; k < 28; ++k) {
        const int d = k & 3;
#pragma unroll
        for (int i = 0; i < 2; ++i)
#pragma unroll
          for (int j = 0; j < 2; ++j)
            acc[i][j] = MFMA32(__builtin_bit_cast(bf16x8, fw[d][j]), __builtin_bit_cast(bf16x8, fa[d][i]), acc[i][j]);
        SBAR();
        if (k + 4 < 28) {
          fa[d][0] = ld_sc1(rws, lane16, sa0 + (k + 4) * 1024); fa[d][1] = ld_sc1(rws, lane16, sa1 + (k + 4) * 1024);
          fw[d][0] = ld_sc1(rws, lane16, sw + (k + 4) * 16384); fw[d][1] = ld_sc1(rws, lane16 + 1024, sw + (k + 4) * 16384);
        } else {
          wof[2 * d][0] = ld_sc1(rws, lane16, so + (2 * d) * 16384); wof[2 * d][1] = ld_sc1(rws, lane16 + 1024, so + (2 * d) * 16384);
          wof[2 * d + 1][0] = ld_sc1(rws, lane16, so + (2 * d + 1) * 16384); wof[2 * d + 1][1] = ld_sc1(rws, lane16 + 1024, so + (2 * d + 1) * 16384);
        }
        SBAR();
      }
    }
    STAMP(7);
    BAR_RAW();
    STAMP(8);
    poll1(ws, WS_OFLAG + ((2 * om + ((lane >> 3) & 1)) * 32 + 8 * g + (lane & 7)) * 8, tok);
    STAMP(9);
    {
      const unsigned so = WS_O + ((2 * om) * 32 + 8 * g) * 1024u, lo = g * 16384;
#pragma unroll
      for (int k = 0; k < 8; ++k) {
        dma_sc1(rws, lane16, so + k * 1024, lo + k * 2048);
        dma_sc1(rws, lane16, so + 32768 + k * 1024, lo + k * 2048 + 1024);
      }
#define WOSTEP(k, n) do { WAITV(n); \
        const bf16x8 o0 = lds_ld<bf16x8>(lo + (k) * 2048 + lane16), o1 = lds_ld<bf16x8>(lo + (k) * 2048 + 1024 + lane16); \
        acc[0][0] = MFMA32(__builtin_bit_cast(bf16x8, wof[k][0]), o0, acc[0][0]); acc[0][1] = MFMA32(__builtin_bit_cast(bf16x8, wof[k][1]), o0, acc[0][1]); \
        acc[1][0] = MFMA32(__builtin_bit_cast(bf16x8, wof[k][0]), o1, acc[1][0]); acc[1][1] = MFMA32(__builtin_bit_cast(bf16x8, wof[k][1]), o1, acc[1][1]); } while (0)
      WOSTEP(0, 14); WOSTEP(1, 12); WOSTEP(2, 10); WOSTEP(3, 8); WOSTEP(4, 6); WOSTEP(5, 4); WOSTEP(6, 2); WOSTEP(7, 0);
#undef WOSTEP
    }
    {
      const unsigned pb = L_PART + g * PART_SZ + (lane & 31) * PART_ROW + (lane >> 5) * 16;
#pragma unroll
      for (int i = 0; i < 2; ++i)
#pragma unroll
        for (int j = 0; j < 2; ++j)
#pragma unroll
          for (int q = 0; q < 4; ++q)
            lds_st<f32x4>(pb + i * 32 * PART_ROW + j * 128 + q * 32, (f32x4){acc[i][j][4 * q], acc[i][j][4 * q + 1], acc[i][j][4 * q + 2], acc[i][j][4 * q + 3]});
    }
    STAMP(10);
    BAR();
    return;
  }

  const int r = lane & 31, h = lane >> 5;
  u64 tok;
  u32x4 actp[8];
  if (wid < 4) {
    const int w = wid;
    if (w == 0) {
      const u64 e = __hip_atomic_load((gu64*)(ws + WS_EPOCH), RLX_AGENT) + 1;
      if (lane == 0) { lds_st<u32x2>(M_TOK, (u32x2){(unsigned)e, (unsigned)(e >> 32)}); lds_st<unsigned>(M_CNT, 0u); lds_st<unsigned>(M_GO, 0u); }
    }
    f32x16 a0 = {}, a1 = {}, a2 = {};
    f32x4 cs = {};
    float sx = 0.f, sxx = 0.f;
    if (w < 2) qkv_mainloop<false>(lane, w, cb, a0, a1, a2, sx, sxx, cs);
    else qkv_mainloop<true>(lane, w, cb, a0, a1, a2, sx, sxx, cs);
    STAMP(1);
    sx = half_sum(sx); sxx = half_sum(sxx);
    const float mu = sx * (1.0f / 512.0f);
    const float var = fmaxf(sxx * (1.0f / 512.0f) - mu * mu, 0.f);
    float rs = __builtin_amdgcn_rsqf(var + 1e-5f), mr = mu * rs;
    if (lane < 32) { lds_st<float>(M_RS + (32 * w + r) * 4, rs); lds_st<float>(M_MR + (32 * w + r) * 4, mr); }
    if (lane < 16 && w < 3) {
      const unsigned d = (w < 2) ? M_CSQK + w * 128 : M_CSV;
      lds_st<float>(d + lane * 4, cs[0]); lds_st<float>(d + 64 + lane * 4, cs[1]);
    }
    BAR();
    STAMP(2);
    tok = lds_tok();
    const int tt = rb * 4 + w;
    {
      float t0[16], t1[16];
      ld_rows(t0, M_RS + 128 * w, h); ld_rows(t1, M_MR + 128 * w, h);
      const float csv = lds_ld<float>(M_CSV + r * 4);
      u32x4 p0, p1;
#pragma unroll
      for (int i = 0; i < 4; ++i) {
        p0[i] = pk2(a2[2 * i] * t0[2 * i] - t1[2 * i] * csv, a2[2 * i + 1] * t0[2 * i + 1] - t1[2 * i + 1] * csv);
        p1[i] = pk2(a2[8 + 2 * i] * t0[8 + 2 * i] - t1[8 + 2 * i] * csv, a2[9 + 2 * i] * t0[9 + 2 * i] - t1[9 + 2 * i] * csv);
      }
      const unsigned vo = (r >> 4) * 65536 + h * 256 + (r & 15) * 16, so = WS_V + (2 * cb * 64 + tt) * 1024u;
      st_sc1(p0, rws, vo, so); st_sc1(p1, rws, vo + 512, so);
    }
    {
      if (cb < 8) { rs *= 0.25f * 1.4426950408889634f; mr *= 0.25f * 1.4426950408889634f; }
      const unsigned base = ((cb < 8) ? WS_Q : WS_K) + ((4 * (cb & 7)) * 64 + tt) * 1024u;
#pragma unroll
      for (int t = 0; t < 2; ++t) {
        const int ct = (w & 1) ^ t;
        const f32x16 a = t ? a1 : a0;
        float c[16];
        ld_rows(c, M_CSQK + 128 * ct, h);
        u32x4 p0, p1;
#pragma unroll
        for (int i = 0; i < 4; ++i) {
          p0[i] = pk2(a[2 * i] * rs - mr * c[2 * i], a[2 * i + 1] * rs - mr * c[2 * i + 1]);
          p1[i] = pk2(a[8 + 2 * i] * rs - mr * c[8 + 2 * i], a[9 + 2 * i] * rs - mr * c[9 + 2 * i]);
        }
        st_sc1(p0, rws, lane16, base + (2 * ct) * 65536u); st_sc1(p1, rws, lane16, base + (2 * ct + 1) * 65536u);
      }
    }
    WAITV(0);
    if (lane == 0) {
      const unsigned old = __hip_atomic_fetch_add((AS3 unsigned*)(uintptr_t)M_CNT, 1u, __ATOMIC_RELAXED, __HIP_MEMORY_SCOPE_WORKGROUP);
      if (old == 3) __hip_atomic_store((gu64*)(ws + WS_QKFLAG + (cb * 16 + rb) * 8), tok, RLX_AGENT);
    }
    STAMP(3);
  } else {
    const int rp = (wid - 4) & 1, cp = (wid - 4) >> 1, q4 = (lane & 15) >> 2;
    if (cb < 14) {
      const float bias = bflo(b1[128 * cb + 64 * cp + 32 * rp + r]);
      unsigned xa[4];
#pragma unroll
      for (int ks = 0; ks < 4; ++ks) xa[ks] = rp * 8192 + r * 128 + ((((h ^ ((r >> 1) & 7))) ^ (2 * ks)) << 4);
      unsigned wa0 = 16384 + (2 + cp) * 8192 + (8 * h + q4) * 128 + ((lane >> 4) & 1) * 32 + (lane & 3) * 8 + ((rp ^ (q4 >> 1)) << 6), wa1 = wa0 ^ 64;
      const unsigned one2 = ((lane & 15) == ((lane >> 4) & 1)) ? 0x3f803f80u : 0u;
      const bf16x8 sel = __builtin_bit_cast(bf16x8, (u32x4){one2, one2, one2, one2});
      f32x16 a00 = {}, a01 = {}, a10 = {}, a11 = {};
      f32x4 cs = {};
      bf16x8 x0A, x1A, w0A, w1A, x0B, x1B, w0B, w1B;
#define FRD(X0, X1, W0, W1, ks) do { X0 = lds_ld<bf16x8>(xa[ks]); W0 = lds_tr2(wa0 + (ks) * 2048, wa0 + (ks) * 2048 + 512); \
    X1 = lds_ld<bf16x8>(xa[ks] + 4096); W1 = lds_tr2(wa1 + (ks) * 2048, wa1 + (ks) * 2048 + 512); SBAR(); } while (0)
#define FMM(X0, X1, W0, W1) do { a00 = MFMA32(W0, X0, a00); a10 = MFMA32(W0, X1, a10); a01 = MFMA32(W1, X0, a01); a11 = MFMA32(W1, X1, a11); \
    cs = MFMA16(sel, W0, cs); SBAR(); } while (0)
      PIN(a00); PIN(a01); PIN(a10); PIN(a11); PIN(cs); SBAR();
      BAR();
      FRD(x0A, x1A, w0A, w1A, 0);
      int slot = 0;
#pragma nounroll
      for (int s = 0; s < 8; ++s) {
        FRD(x0B, x1B, w0B, w1B, 1); FMM(x0A, x1A, w0A, w1A);
        FRD(x0A, x1A, w0A, w1A, 2); FMM(x0B, x1B, w0B, w1B);
        FRD(x0B, x1B, w0B, w1B, 3); FMM(x0A, x1A, w0A, w1A);
        WAITL0(); SBAR();
        if (s < 7) {
          const int d = (slot == 2) ? -2 * (int)SLOT : (int)SLOT;
          slot = (slot == 2) ? 0 : slot + 1;
          xa[0] += d; xa[1] += d; xa[2] += d; xa[3] += d; wa0 += d; wa1 += d;
          BAR();
          FRD(x0A, x1A, w0A, w1A, 0);
        }
        FMM(x0B, x1B, w0B, w1B);
      }
#undef FRD
#undef FMM
      STAMP(1);
      {
        const unsigned t = (2 * cp + rp) * 128;
        if (lane < 16) { lds_st<float>(M_CSF + t + lane * 4, cs[0]); lds_st<float>(M_CSF + t + 64 + lane * 4, cs[1]); }
        if (lane < 32) lds_st<float>(M_B1 + t + lane * 4, bias);
      }
      BAR();
      STAMP(2);
      tok = lds_tok();
      float rs[2], mr[2];
#pragma unroll
      for (int i = 0; i < 2; ++i) { rs[i] = lds_ld<float>(M_RS + (64 * rp + 32 * i + r) * 4); mr[i] = -lds_ld<float>(M_MR + (64 * rp + 32 * i + r) * 4); }
#pragma unroll
      for (int j = 0; j < 2; ++j) {
        const int ct = 2 * cp + (rp ^ j);
        float c[16], bb[16];
        ld_rows(c, M_CSF + ct * 128, h); ld_rows(bb, M_B1 + ct * 128, h);
#pragma unroll
        for (int i = 0; i < 2; ++i) {
          const f32x16 a = j ? (i ? a11 : a01) : (i ? a10 : a00);
          unsigned p[8];
#pragma unroll
          for (int e = 0; e < 8; ++e) {
            const f32x2 off = __builtin_elementwise_fma((f32x2){c[2 * e], c[2 * e + 1]}, (f32x2){mr[i], mr[i]}, (f32x2){bb[2 * e], bb[2 * e + 1]});
            const f32x2 hv = __builtin_elementwise_fma((f32x2){a[2 * e], a[2 * e + 1]}, (f32x2){rs[i], rs[i]}, off);
            const f32x2 m = mish2(hv);
            p[e] = pk2(m[0], m[1]);
          }
          actp[(j * 2 + i) * 2] = (u32x4){p[0], p[1], p[2], p[3]}; actp[(j * 2 + i) * 2 + 1] = (u32x4){p[4], p[5], p[6], p[7]};
        }
      }
    } else {
      const int wgi = (cb - 14) * 16 + rb;
      const unsigned wl = 16384 + (2 + cp) * 8192 + (4 * h + q4) * 128 + ((lane >> 4) & 1) * 32 + (lane & 3) * 8 + ((q4 >> 1) << 6) + rp * 4096;
#pragma nounroll
      for (int i = 0; i < 8; ++i) {
        BAR();
        const int u = wgi + 32 * i;
        if (u < 144) {
          const unsigned a = wl + (unsigned)(i % 3) * SLOT;
#pragma unroll
          for (int k = 0; k < 2; ++k)
#pragma unroll
            for (int ct = 0; ct < 2; ++ct) {
              const unsigned aa = (a ^ (ct << 6)) + k * 2048;
              const bf16x8 f = lds_tr2(aa, aa + 1024);
              st_sc1(__builtin_bit_cast(u32x4, f), rws, lane16, WS_W2F + ((4 * (u >> 2) + 2 * rp + k) * 16 + 4 * (u & 3) + 2 * cp + ct) * 1024u);
            }
        }
      }
      BAR();
      tok = lds_tok();
    }
    STAMP(3);
  }

  f32x16 o = {};
  bf16x8 v0, v1 = {};
  u32x4 p0, p1 = {0, 0, 0, 0};
  PIN(o); PIN(v1); PIN(p1); SBAR();
  BAR_RAW();
  STAMP(4);
  if (wid >= 4 && cb < 14) {
    const int rp = (wid - 4) & 1, cp = (wid - 4) >> 1;
#pragma unroll
    for (int j = 0; j < 2; ++j)
#pragma unroll
      for (int i = 0; i < 2; ++i) {
        const unsigned so = WS_ACT + ((rb * 4 + 2 * rp + i) * 112 + 8 * cb + 2 * (2 * cp + (rp ^ j))) * 1024u;
        st_sc1(actp[(j * 2 + i) * 2], rws, lane16, so); st_sc1(actp[(j * 2 + i) * 2 + 1], rws, lane16 + 1024, so);
      }
  }
  {
    const bf16x8 q = lds_ld<bf16x8>(L_Q + wid * 1024 + lane16);
    unsigned kp = L_K + lane16;
    const bool real = (lane & 16) == 0;
    const unsigned vp0 = (real ? L_V : L_ONES) + h * 256 + (lane & 15) * 16;
    unsigned vp = vp0;
    const unsigned vinc = real ? 4096u : 0u;
    const f32x16 zero = {};
    bf16x8 kn = lds_ld<bf16x8>(kp);
    f32x16 sA = MFMA32(kn, q, zero), sB;
    kn = lds_ld<bf16x8>(kp + 1024);
    float m = fmaxf(sA[0], sA[1]);
#pragma unroll
    for (int i = 2; i < 16; i += 2) m = fmaxf(fmaxf(m, sA[i]), sA[i + 1]);
    m = half_max(m);
    f32x16 negm;
#pragma unroll
    for (int i = 0; i < 16; ++i) { negm[i] = -m; sA[i] -= m; }
    PIN(negm);
    __builtin_amdgcn_s_setprio(2);
#define E2(S, i) do { S[i] = EX2(S[i]); S[i + 1] = EX2(S[i + 1]); } while (0)
#define STEP(SC, SN, u) do { \
    E2(SC, 0); PIN(SC); SBAR(); \
    o = MFMA32(v1, __builtin_bit_cast(bf16x8, p1), o); PIN(o); SBAR(); \
    v0 = lds_ld<bf16x8>(vp + (u) * 1024); v1 = lds_ld<bf16x8>(vp + (u) * 1024 + 512); SBAR(); \
    E2(SC, 2); E2(SC, 4); PIN(SC); SBAR(); \
    SN = MFMA32(kn, q, negm); PIN(SN); SBAR(); \
    kn = lds_ld<bf16x8>(kp + ((u) + 2) * 1024); SBAR(); \
    E2(SC, 6); E2(SC, 8); \
    p0[0] = pk2(SC[0], SC[1]); p0[1] = pk2(SC[2], SC[3]); p0[2] = pk2(SC[4], SC[5]); p0[3] = pk2(SC[6], SC[7]); PIN(p0); PIN(SC); SBAR(); \
    E2(SC, 10); PIN(SC); SBAR(); \
    o = MFMA32(v0, __builtin_bit_cast(bf16x8, p0), o); PIN(o); SBAR(); \
    E2(SC, 12); E2(SC, 14); \
    p1[0] = pk2(SC[8], SC[9]); p1[1] = pk2(SC[10], SC[11]); p1[2] = pk2(SC[12], SC[13]); p1[3] = pk2(SC[14], SC[15]); PIN(p1); SBAR(); \
  } while (0)
#pragma nounroll
    for (int it = 0; it < 16; ++it) {
      PIN(kp); PIN(vp);
      STEP(sA, sB, 0); STEP(sB, sA, 1); STEP(sA, sB, 2); STEP(sB, sA, 3);
      kp += 4096; vp += vinc;
      if ((0x480a >> it) & 1) {
        if (it == 1) BAR_RAW();
        else if (it == 3) { if (wid >= 4) publish(ws, WS_ACTFLAG + ((rb * 2 + (wid & 1)) * 32 + cb * 2 + ((wid - 4) >> 1)) * 8, tok, lane); }
        else if (it == 11) __builtin_amdgcn_s_setprio(1);
        else __builtin_amdgcn_s_setprio(0);
      }
    }
#undef STEP
#undef E2
    o = MFMA32(v1, __builtin_bit_cast(bf16x8, p1), o);
    STAMP(5);
    float chk = o[8];
#pragma unroll
    for (int i = 0; i < 8; ++i) chk += fabsf(o[i]) * 0x1p-40f;
    if (__any(!(chk < 0x1p64f))) {
      float mm = -3.0e38f;
      o = zero;
#pragma nounroll
      for (int t = 0; t < 64; ++t) {
        f32x16 s = MFMA32(lds_ld<bf16x8>(L_K + lane16 + t * 1024), q, zero);
        float tm = s[0];
#pragma unroll
        for (int i = 1; i < 16; ++i) tm = fmaxf(tm, s[i]);
        const float mn = fmaxf(mm, half_max(tm));
        const float al = EX2(mm - mn);
        mm = mn;
        u32x4 pa, pb;
#pragma unroll
        for (int i = 0; i < 4; ++i) {
          pa[i] = pk2(EX2(s[2 * i] - mn), EX2(s[2 * i + 1] - mn));
          pb[i] = pk2(EX2(s[8 + 2 * i] - mn), EX2(s[9 + 2 * i] - mn));
        }
#pragma unroll
        for (int i = 0; i < 16; ++i) o[i] *= al;
        const unsigned va = vp0 + (real ? t * 1024 : 0);
        o = MFMA32(lds_ld<bf16x8>(va), __builtin_bit_cast(bf16x8, pa), o);
        o = MFMA32(lds_ld<bf16x8>(va + 512), __builtin_bit_cast(bf16x8, pb), o);
      }
    }
    const float rl = __builtin_amdgcn_rcpf(o[8]);
    const u32x4 ov = {pk2(o[0] * rl, o[1] * rl), pk2(o[2] * rl, o[3] * rl), pk2(o[4] * rl, o[5] * rl), pk2(o[6] * rl, o[7] * rl)};
    const int qt = qb * 8 + wid;
    st_sc1(ov, rws, lane16, WS_O + (qt * 32 + head) * 1024u);
    publish(ws, WS_OFLAG + (qt * 32 + head) * 8, tok, lane);
    STAMP(6);
    BAR();
  }
  {
    const int row = tid >> 3, c8 = tid & 7;
    const size_t gi = (size_t)(64 * om + row) * 512 + 64 * on + 8 * c8;
    const u32x4 xv = *(const u32x4*)(x + gi);
    BAR_RAW();
    STAMP(7);
    f32x4 s0 = {bflo(xv[0]), bfhi(xv[0]), bflo(xv[1]), bfhi(xv[1])}, s1 = {bflo(xv[2]), bfhi(xv[2]), bflo(xv[3]), bfhi(xv[3])};
    const unsigned pa = L_PART + row * PART_ROW + c8 * 32;
#pragma unroll
    for (int g = 0; g < 4; ++g) { s0 += lds_ld<f32x4>(pa + g * PART_SZ); s1 += lds_ld<f32x4>(pa + g * PART_SZ + 16); }
    *(u32x4*)(out + gi) = (u32x4){pk2(s0[0], s0[1]), pk2(s0[2], s0[3]), pk2(s1[0], s1[1]), pk2(s1[2], s1[3])};
    if (blockIdx.x == 0 && tid == 0) __hip_atomic_store((gu64*)(ws + WS_EPOCH), tok, RLX_AGENT);
    STAMP(8);
  }
}

extern "C" void kernel_launch(void* const* d_in, const int* in_sizes, int n_in, void* d_out, int out_size, void* d_ws,
                              size_t ws_size, hipStream_t stream) {
  fused_block<<<256, 768, 0, stream>>>((const uint16_t*)d_in[0], (const uint16_t*)d_in[1], (const uint16_t*)d_in[2],
                                       (const uint16_t*)d_in[4], (const uint16_t*)d_in[5], (const uint16_t*)d_in[6],
                                       (uint16_t*)d_out, (char*)d_ws);
}
```

```cpp
#define PADAT 0
#include <hip/hip_runtime.h>
#include <stdint.h>

typedef __attribute__((ext_vector_type(8))) __bf16 bf16x8;
typedef __attribute__((ext_vector_type(2))) __bf16 bf16x2;
typedef __attribute__((ext_vector_type(2))) float f32x2;
typedef __attribute__((ext_vector_type(4))) float f32x4;
typedef __attribute__((ext_vector_type(16))) float f32x16;
typedef __attribute__((ext_vector_type(2))) unsigned u32x2;
typedef __attribute__((ext_vector_type(4))) unsigned u32x4;
typedef __attribute__((ext_vector_type(4))) short s16x4;
typedef unsigned long long u64;
typedef __attribute__((address_space(1))) u64 gu64;
typedef __amdgpu_buffer_rsrc_t rsrc_t;
#define AS3 __attribute__((address_space(3)))
#define DI __device__ __forceinline__

constexpr unsigned WS_EPOCH = 0;
constexpr unsigned WS_QKFLAG = 4096;
constexpr unsigned WS_ACTFLAG = WS_QKFLAG + 8192;
constexpr unsigned WS_OFLAG = WS_ACTFLAG + 8192;
constexpr unsigned WS_STAMP = 65536;
constexpr unsigned WS_Q = 1u << 20;
constexpr unsigned WS_K = WS_Q + (2u << 20);
constexpr unsigned WS_V = WS_K + (2u << 20);
constexpr unsigned WS_O = WS_V + (2u << 20);
constexpr unsigned WS_ACT = WS_O + (2u << 20);
constexpr unsigned WS_W2F = WS_ACT + 64u * 112u * 1024u;
constexpr unsigned WS_END = WS_W2F + 144u * 16u * 1024u;

constexpr unsigned SLOT = 32768;
constexpr unsigned L_K = 0, L_V = 65536, L_ONES = 131072, L_Q = 135168;
constexpr unsigned L_PART = 65536, PART_ROW = 272, PART_SZ = 64 * PART_ROW;
constexpr unsigned L_MISC = 159744;
constexpr unsigned M_CSQK = L_MISC, M_CSV = M_CSQK + 256, M_CSF = M_CSV + 256,
                   M_CNT = M_CSF + 512, M_CNT2 = M_CNT + 4, M_GO = M_CNT + 8, M_ZEND = M_CNT + 16,
                   M_RS = L_MISC + 2048, M_MR = M_RS + 512, M_B1 = M_MR + 512, M_TOK = M_B1 + 512;
constexpr unsigned LDS_BYTES = 163840;

constexpr unsigned SPIN_CAP = 40000;
#define PUBSTAGE 1

#define MFMA32(a, b, c) __builtin_amdgcn_mfma_f32_32x32x16_bf16(a, b, c, 0, 0, 0)
#define MFMA16(a, b, c) __builtin_amdgcn_mfma_f32_16x16x32_bf16(a, b, c, 0, 0, 0)
#define SBAR() __builtin_amdgcn_sched_barrier(0)
#define PIN(x) asm volatile("" : "+v"(x))
#define BAR() asm volatile("s_waitcnt lgkmcnt(0)\n\ts_barrier" ::: "memory")
#define WAITL0() __builtin_amdgcn_s_waitcnt(0xC07F)
#define BAR_RAW() asm volatile("s_barrier" ::: "memory")
#define WAITV(n) asm volatile("s_waitcnt vmcnt(" #n ")" ::: "memory")
#define RLX_AGENT __ATOMIC_RELAXED, __HIP_MEMORY_SCOPE_AGENT
#define EX2(x) __builtin_amdgcn_exp2f(x)

#define STAMP(i) do { if ((i) == PADAT) tpad = __builtin_amdgcn_s_memrealtime(); } while (0)
#define LSTAMP(i) do { } while (0)
#define LFLUSH() do { } while (0)

template <typename T> DI T lds_ld(unsigned a) { return *(const AS3 T*)(uintptr_t)a; }
template <typename T> DI void lds_st(unsigned a, T v) { *(AS3 T*)(uintptr_t)a = v; }
DI bf16x8 lds_tr2(unsigned a0, unsigned a1) {
  s16x4 lo = __builtin_amdgcn_ds_read_tr16_b64_v4i16((AS3 s16x4*)(uintptr_t)a0);
  s16x4 hi = __builtin_amdgcn_ds_read_tr16_b64_v4i16((AS3 s16x4*)(uintptr_t)a1);
  return __builtin_bit_cast(bf16x8, __builtin_shufflevector(lo, hi, 0, 1, 2, 3, 4, 5, 6, 7));
}
DI unsigned pk2(float a, float b) { f32x2 v = {a, b}; return __builtin_bit_cast(unsigned, __builtin_convertvector(v, bf16x2)); }
DI float bflo(unsigned u) { return __uint_as_float(u << 16); }
DI float bfhi(unsigned u) { return __uint_as_float(u & 0xffff0000u); }
DI float half_sum(float v) { auto r = __builtin_amdgcn_permlane32_swap(__float_as_uint(v), __float_as_uint(v), false, false); return __uint_as_float(r[0]) + __uint_as_float(r[1]); }
DI float half_max(float v) { auto r = __builtin_amdgcn_permlane32_swap(__float_as_uint(v), __float_as_uint(v), false, false); return fmaxf(__uint_as_float(r[0]), __uint_as_float(r[1])); }
DI unsigned rfl(unsigned v) { return __builtin_amdgcn_readfirstlane(v); }
DI void dma(rsrc_t r, unsigned voff, unsigned soff, unsigned ldsdst) {
  asm volatile("s_mov_b32 m0, %0\n\ts_nop 0\n\tbuffer_load_dwordx4 %1, %2, %3 offen lds" ::"s"(ldsdst), "v"(voff), "s"(r), "s"(soff) : "memory");
}
DI void dma_sc1(rsrc_t r, unsigned voff, unsigned soff, unsigned ldsdst) {
  asm volatile("s_mov_b32 m0, %0\n\ts_nop 0\n\tbuffer_load_dwordx4 %1, %2, %3 offen sc1 lds" ::"s"(ldsdst), "v"(voff), "s"(r), "s"(soff) : "memory");
}
DI void st_sc1(u32x4 v, rsrc_t r, unsigned voff, unsigned soff) { __builtin_amdgcn_raw_buffer_store_b128(v, r, voff, soff, 16); }
DI u32x4 ld_sc1(rsrc_t r, unsigned voff, unsigned soff) { return __builtin_amdgcn_raw_buffer_load_b128(r, voff, soff, 16); }
DI void publish(char* ws, unsigned off, u64 tok, int lane) {
  WAITV(0);
  if (lane == 0) __hip_atomic_store((gu64*)(ws + off), tok, RLX_AGENT);
}
DI void poll1(char* ws, unsigned off, u64 tok) {
#pragma nounroll
  for (unsigned spins = 0; spins < SPIN_CAP; ++spins) {
    const u64 a = __hip_atomic_load((gu64*)(ws + off), RLX_AGENT);
    if (__all(a == tok)) break;
    __builtin_amdgcn_s_sleep(2);
  }
  asm volatile("" ::: "memory");
}
DI u64 lds_tok() { const u32x2 t = lds_ld<u32x2>(M_TOK); return ((u64)rfl(t[1]) << 32) | rfl(t[0]); }

DI void ld_rows(float (&d)[16], unsigned base, int h) {
#pragma unroll
  for (int g = 0; g < 4; ++g) {
    const f32x4 v = lds_ld<f32x4>(base + 16 * h + 32 * g);
    d[4 * g] = v[0]; d[4 * g + 1] = v[1]; d[4 * g + 2] = v[2]; d[4 * g + 3] = v[3];
  }
}

DI void publish_wg(char* ws, unsigned off, unsigned cnt, u64 tok, int lane) {
  WAITV(0);
  if (lane == 0) {
    const unsigned old = __hip_atomic_fetch_add((AS3 unsigned*)(uintptr_t)cnt, 1u, __ATOMIC_RELAXED, __HIP_MEMORY_SCOPE_WORKGROUP);
    if (old == 7) __hip_atomic_store((gu64*)(ws + off), tok, RLX_AGENT);
  }
}

template <int MODE>
DI void gemm_pass(int lane, unsigned xb, unsigned wb, int ct, int pr, f32x16& a0, f32x16& a1, f32x4& cs, float& sx, float& sxx,
                  char* ws = nullptr, unsigned flag = 0, u64 tok = 0) {
  const int r = lane & 31, h = lane >> 5, q4 = (lane & 15) >> 2;
  unsigned xa[4];
#pragma unroll
  for (int j = 0; j < 4; ++j) xa[j] = xb + r * 128 + (((h ^ ((r >> 1) & 7)) ^ (2 * (j ^ pr))) << 4);
  const unsigned wl = wb + (8 * h + q4) * 128 + ((lane >> 4) & 1) * 32 + (lane & 3) * 8 + ((ct ^ (q4 >> 1)) << 6);
  unsigned wA = wl + pr * 2048, wB = wl + (pr ^ 1) * 2048, uA = wA ^ 64, uB = wB ^ 64;
  const unsigned one2 = ((lane & 15) == ((lane >> 4) & 1)) ? 0x3f803f80u : 0u;
  const bf16x8 sel = __builtin_bit_cast(bf16x8, (u32x4){one2, one2, one2, one2});
  bf16x8 xP[4], yP[4], wP[4], xQ[4], yQ[4], wQ[4];
  float sx1 = 0.f, sxx1 = 0.f;
#define RDX(X, j) X[j] = lds_ld<bf16x8>(xa[j])
#define RDW(W, j) W[j] = lds_tr2((((j) & 1) ? wB : wA) + ((j) >> 1) * 4096, (((j) & 1) ? wB : wA) + ((j) >> 1) * 4096 + 512)
#define RDY(Y, j) do { if (MODE == 0) Y[j] = lds_tr2((((j) & 1) ? uB : uA) + ((j) >> 1) * 4096, (((j) & 1) ? uB : uA) + ((j) >> 1) * 4096 + 512); \
                       if (MODE == 2) Y[j] = lds_ld<bf16x8>(xa[j] + 4096); } while (0)
#define RDALL(X, Y, W, j) do { RDX(X, j); RDW(W, j); RDY(Y, j); } while (0)
#define QST(X, i) do { const unsigned u_ = __builtin_bit_cast(u32x4, X)[i]; const float l_ = bflo(u_), h_ = bfhi(u_); \
    sx += l_; sx1 += h_; sxx = __builtin_fmaf(l_, l_, sxx); sxx1 = __builtin_fmaf(h_, h_, sxx1); PIN(sx); PIN(sx1); PIN(sxx); PIN(sxx1); } while (0)
#define MM0(X, Y, W, j) do { if (MODE == 1) a0 = MFMA32(X[j], W[j], a0); else a0 = MFMA32(W[j], X[j], a0); SBAR(); } while (0)
#define MM1(X, Y, W, j) do { if (MODE == 0) a1 = MFMA32(Y[j], X[j], a1); if (MODE == 2) a1 = MFMA32(W[j], Y[j], a1); \
    if (MODE == 1) { QST(X[j], 0); QST(X[j], 1); QST(X[j], 2); QST(X[j], 3); } SBAR(); } while (0)
#define MMC(W, j) do { cs = MFMA16(sel, W[j], cs); SBAR(); } while (0)
#define GSTAGE(X, Y, W, XN, YN, WN, RD, d) do { \
    WAITL0(); SBAR(); \
    if (RD) { xa[0] += d; xa[1] += d; xa[2] += d; xa[3] += d; wA += d; wB += d; if (MODE == 0) { uA += d; uB += d; } SBAR(); BAR_RAW(); SBAR(); } \
    MM0(X, Y, W, 0); if (RD) RDALL(XN, YN, WN, 0); SBAR(); MM1(X, Y, W, 0); if (RD) RDALL(XN, YN, WN, 1); SBAR(); \
    MM0(X, Y, W, 1); if (RD) RDALL(XN, YN, WN, 2); SBAR(); MM1(X, Y, W, 1); if (RD) RDALL(XN, YN, WN, 3); SBAR(); \
    MMC(W, 0); MM0(X, Y, W, 2); MM1(X, Y, W, 2); MMC(W, 2); MM0(X, Y, W, 3); MM1(X, Y, W, 3); } while (0)
  RDALL(xP, yP, wP, 0); RDALL(xP, yP, wP, 1); RDALL(xP, yP, wP, 2); RDALL(xP, yP, wP, 3); SBAR();
#pragma nounroll
  for (int s = 0; s < 3; ++s) {
    GSTAGE(xP, yP, wP, xQ, yQ, wQ, true, (int)SLOT);
    const int d2 = (s == 1) ? -3 * (int)SLOT : (int)SLOT;
    GSTAGE(xQ, yQ, wQ, xP, yP, wP, true, d2);
    if (MODE == 2 && s == PUBSTAGE) publish_wg(ws, flag, M_CNT, tok, lane);
  }
  GSTAGE(xP, yP, wP, xQ, yQ, wQ, true, (int)SLOT);
  GSTAGE(xQ, yQ, wQ, xP, yP, wP, false, 0);
  if (MODE == 1) { sx += sx1; sxx += sxx1; }
#undef RDX
#undef RDW
#undef RDY
#undef RDALL
#undef QST
#undef MM0
#undef MM1
#undef MMC
#undef GSTAGE
}
DI void cs_add(unsigned dst, const f32x4& cs, int lane) {
  if (lane < 16) {
    __hip_atomic_fetch_add((AS3 float*)(uintptr_t)(dst + lane * 4), cs[0], __ATOMIC_RELAXED, __HIP_MEMORY_SCOPE_WORKGROUP);
    __hip_atomic_fetch_add((AS3 float*)(uintptr_t)(dst + 64 + lane * 4), cs[1], __ATOMIC_RELAXED, __HIP_MEMORY_SCOPE_WORKGROUP);
  }
}
DI f32x2 mish2(f32x2 z) {
  f32x2 u; u[0] = EX2(z[0]); u[1] = EX2(z[1]);
  u += 1.0f;
  const f32x2 d = __builtin_elementwise_fma(u, u, (f32x2){1.0f, 1.0f});
  f32x2 rc; rc[0] = __builtin_amdgcn_rcpf(d[0]); rc[1] = __builtin_amdgcn_rcpf(d[1]);
  return z * __builtin_elementwise_fma(rc, (f32x2){-1.3862943611198906f, -1.3862943611198906f}, (f32x2){0.6931471805599453f, 0.6931471805599453f});
}

__global__ __launch_bounds__(768) void fused_block(const uint16_t* __restrict__ x, const uint16_t* __restrict__ wqkv,
                                                   const uint16_t* __restrict__ wo, const uint16_t* __restrict__ w1,
                                                   const uint16_t* __restrict__ b1, const uint16_t* __restrict__ w2,
                                                   uint16_t* __restrict__ out, char* ws) {
  __shared__ __attribute__((aligned(1024))) char smem[LDS_BYTES];
  asm volatile("" ::"v"((AS3 char*)smem) : "memory");
  const int tid = threadIdx.x, lane = tid & 63;
  const int wid = rfl(tid >> 6);
  const int xcd = blockIdx.x & 7, jb = blockIdx.x >> 3;
  const int rb = (xcd & 1) * 8 + (jb & 7), cb = (xcd >> 1) * 4 + (jb >> 3);
  const int head = xcd * 4 + (jb & 3), qb = jb >> 2;
  const int om = xcd * 4 + (jb & 3), on = jb >> 2;
  const rsrc_t rws = __builtin_amdgcn_make_buffer_rsrc(ws, 0, WS_END, 0x00020000);
  const unsigned lane16 = lane * 16;
  asm volatile("" ::"s"(x), "s"(wqkv), "s"(wo), "s"(w1), "s"(b1), "s"(w2), "s"(out), "s"(ws));
  u64 tpad = 0;
  STAMP(0);

  if (wid >= 8) {
    const int g = wid - 8;
    const int psel = lane >> 4;
    const unsigned pflag = WS_QKFLAG + (((psel == 0) ? 8 + (head >> 2) : (psel == 1) ? (head >> 1) : (head >> 2)) * 16 +
                                        ((psel < 2) ? (lane & 15) : 2 * qb + (lane & 1))) * 8;
    u64 tok;
    {
      const rsrc_t rx = __builtin_amdgcn_make_buffer_rsrc((void*)x, 0, 2048 * 512 * 2, 0x00020000);
      const unsigned vx0 = (lane >> 3) * 1024 + (((lane & 7) ^ (lane >> 4)) << 4), vx1 = vx0 ^ 64;
      const unsigned vwc = ((lane & 7) ^ (((lane >> 4) & 1) << 2)) << 4;
      const int un = g >> 1, wgi = (cb - 14) * 16 + rb;
#pragma nounroll
      for (int i = 0; i < 16; ++i) {
        if (i >= 2 && i != 3) {
          if (i == 2) WAITV(8); else WAITV(16);
          BAR_RAW();
          LSTAMP(i == 2 ? 0 : i - 3);
        }
        const int s = i & 7;
        const unsigned slot = (unsigned)(i & 3) * SLOT;
        const void* wp; unsigned wbytes, stride, wsoff;
        if (i < 8) { wp = wqkv; wbytes = 512u * 3072u; stride = 3072u; wsoff = (un ? 2048u + 128u * (cb >> 1) : 128u * cb) + 64u * s * 3072u; }
        else if (cb < 14) { wp = w1; wbytes = 512u * 3584u; stride = 3584u; wsoff = 256u * cb + 128u * un + 64u * s * 3584u; }
        else {
          const int u = min(wgi + 32 * max(s - 3, 0), 143);
          wp = (u < 112) ? (const void*)w2 : (const void*)wo;
          wbytes = (u < 112) ? 1792u * 1024u : 512u * 1024u; stride = 1024u;
          wsoff = (unsigned)(((u < 112) ? u : u - 112) >> 2) * 65536u + (unsigned)(u & 3) * 256u + 128u * un;
        }
        const rsrc_t rw = __builtin_amdgcn_make_buffer_rsrc((void*)wp, 0, wbytes, 0x00020000);
        const unsigned vw = (lane >> 3) * stride + vwc;
        wsoff += 32u * (g & 1) * stride;
#pragma unroll
        for (int p = 0; p < 4; ++p) {
          dma(rx, (p & 1) ? vx1 : vx0, (unsigned)(128 * rb + 32 * g + 8 * p) * 1024u + 128u * s, slot + (32 * g + 8 * p) * 128);
          dma(rw, vw, wsoff + 8u * p * stride, slot + 16384 + g * 4096 + p * 1024);
        }
      }
      { const unsigned one = (lane & 11) ? 0u : 0x3f803f80u;
        lds_st<u32x4>(L_ONES + g * 1024 + lane16, (u32x4){one, one, one, one}); }
      tok = lds_tok();
      WAITV(16); BAR_RAW();
      LSTAMP(13);
      WAITV(8); BAR_RAW();
      LSTAMP(14);
      WAITV(0); BAR_RAW();
      LSTAMP(15);
      if (g == 0) {
        const u64 a = __hip_atomic_load((gu64*)(ws + pflag), RLX_AGENT);
        if (__all(a == tok)) lds_st<unsigned>(M_GO, 1u);
      }
      BAR();
      LSTAMP(16); LFLUSH();
    }
    STAMP(2);
    if (!rfl(*(volatile AS3 unsigned*)(uintptr_t)M_GO)) {
      if (g == 0) {
        poll1(ws, pflag, tok);
        lds_st<unsigned>(M_GO, 1u);
      } else {
#pragma nounroll
        for (unsigned spins = 0; spins < 64 * SPIN_CAP; ++spins) {
          if (rfl(*(volatile AS3 unsigned*)(uintptr_t)M_GO)) break;
          __builtin_amdgcn_s_sleep(1);
        }
      }
    }
    asm volatile("" ::: "memory");
    STAMP(3);
    {
      const unsigned hq = WS_Q + head * 65536u + qb * 8192u, hk = WS_K + head * 65536u, hv = WS_V + head * 65536u;
      dma_sc1(rws, lane16, hq + (2 * g) * 1024, L_Q + (2 * g) * 1024);
      dma_sc1(rws, lane16, hq + (2 * g + 1) * 1024, L_Q + (2 * g + 1) * 1024);
#pragma unroll
      for (int i = 0; i < 16; ++i) {
        const unsigned t = (unsigned)(4 * i + g) * 1024u;
        dma_sc1(rws, lane16, hk + t, L_K + t);
        dma_sc1(rws, lane16, hv + t, L_V + t);
      }
    }
    WAITV(26); BAR();
    STAMP(4);
    WAITV(0); BAR_RAW();
    STAMP(5);
    __builtin_amdgcn_s_setprio(3);
    {
      const int wg = (on >> 1) + 4 * (lane & 7);
      poll1(ws, WS_ACTFLAG + ((lane < 32) ? (om >> 1) * 16 + min(lane, 13) : (wg & 15) * 16 + 14 + (wg >> 4)) * 8, tok);
    }
    STAMP(6);
    f32x16 acc[2][2] = {};
    u32x4 wof[8][2];
    {
      const unsigned sa0 = WS_ACT + ((2 * om) * 112 + 28 * g) * 1024u, sa1 = sa0 + 112 * 1024u;
      const unsigned sw = WS_W2F + (28 * g * 16 + 2 * on) * 1024u;
      const unsigned so = WS_W2F + ((112 + 8 * g) * 16 + 2 * on) * 1024u;
      u32x4 fa[4][2], fw[4][2];
#pragma unroll
      for (int d = 0; d < 4; ++d) {
        fa[d][0] = ld_sc1(rws, lane16, sa0 + d * 1024); fa[d][1] = ld_sc1(rws, lane16, sa1 + d * 1024);
        fw[d][0] = ld_sc1(rws, lane16, sw + d * 16384); fw[d][1] = ld_sc1(rws, lane16 + 1024, sw + d * 16384);
      }
      SBAR();
#pragma unroll
      for (int k = 0; k < 28; ++k) {
        const int d = k & 3;
#pragma unroll
        for (int i = 0; i < 2; ++i)
#pragma unroll
          for (int j = 0; j < 2; ++j)
            acc[i][j] = MFMA32(__builtin_bit_cast(bf16x8, fw[d][j]), __builtin_bit_cast(bf16x8, fa[d][i]), acc[i][j]);
        SBAR();
        if (k + 4 < 28) {
          fa[d][0] = ld_sc1(rws, lane16, sa0 + (k + 4) * 1024); fa[d][1] = ld_sc1(rws, lane16, sa1 + (k + 4) * 1024);
          fw[d][0] = ld_sc1(rws, lane16, sw + (k + 4) * 16384); fw[d][1] = ld_sc1(rws, lane16 + 1024, sw + (k + 4) * 16384);
        } else {
          wof[2 * d][0] = ld_sc1(rws, lane16, so + (2 * d) * 16384); wof[2 * d][1] = ld_sc1(rws, lane16 + 1024, so + (2 * d) * 16384);
          wof[2 * d + 1][0] = ld_sc1(rws, lane16, so + (2 * d + 1) * 16384); wof[2 * d + 1][1] = ld_sc1(rws, lane16 + 1024, so + (2 * d + 1) * 16384);
        }
        SBAR();
      }
    }
    STAMP(7);
    BAR_RAW();
    STAMP(8);
    poll1(ws, WS_OFLAG + ((2 * om + ((lane >> 3) & 1)) * 32 + 8 * g + (lane & 7)) * 8, tok);
    STAMP(9);
    {
      const unsigned so = WS_O + ((2 * om) * 32 + 8 * g) * 1024u, lo = g * 16384;
#pragma unroll
      for (int k = 0; k < 8; ++k) {
        dma_sc1(rws, lane16, so + k * 1024, lo + k * 2048);
        dma_sc1(rws, lane16, so + 32768 + k * 1024, lo + k * 2048 + 1024);
      }
#define WOSTEP(k, n) do { WAITV(n); \
        const bf16x8 o0 = lds_ld<bf16x8>(lo + (k) * 2048 + lane16), o1 = lds_ld<bf16x8>(lo + (k) * 2048 + 1024 + lane16); \
        acc[0][0] = MFMA32(__builtin_bit_cast(bf16x8, wof[k][0]), o0, acc[0][0]); acc[0][1] = MFMA32(__builtin_bit_cast(bf16x8, wof[k][1]), o0, acc[0][1]); \
        acc[1][0] = MFMA32(__builtin_bit_cast(bf16x8, wof[k][0]), o1, acc[1][0]); acc[1][1] = MFMA32(__builtin_bit_cast(bf16x8, wof[k][1]), o1, acc[1][1]); } while (0)
      WOSTEP(0, 14); WOSTEP(1, 12); WOSTEP(2, 10); WOSTEP(3, 8); WOSTEP(4, 6); WOSTEP(5, 4); WOSTEP(6, 2); WOSTEP(7, 0);
#undef WOSTEP
    }
    {
      const unsigned pb = L_PART + g * PART_SZ + (lane & 31) * PART_ROW + (lane >> 5) * 16;
#pragma unroll
      for (int i = 0; i < 2; ++i)
#pragma unroll
        for (int j = 0; j < 2; ++j)
#pragma unroll
          for (int q = 0; q < 4; ++q)
            lds_st<f32x4>(pb + i * 32 * PART_ROW + j * 128 + q * 32, (f32x4){acc[i][j][4 * q], acc[i][j][4 * q + 1], acc[i][j][4 * q + 2], acc[i][j][4 * q + 3]});
    }
    STAMP(10);
    BAR();
    return;
  }

  const int r = lane & 31, h = lane >> 5;
  u64 tok;
  u32x4 actp[4];
  const float LOG2E = 1.4426950408889634f;
  {
    if (wid == 0) {
      const u64 e = __hip_atomic_load((gu64*)(ws + WS_EPOCH), RLX_AGENT) + 1;
      if (lane == 0) lds_st<u32x2>(M_TOK, (u32x2){(unsigned)e, (unsigned)(e >> 32)});
    } else if (wid == 1) {
      lds_st<u32x4>(L_MISC + lane16, (u32x4){0, 0, 0, 0});
      if (lane == 0) lds_st<u32x4>(M_CNT, (u32x4){0, 0, 0, 0});
    } else if (wid < 4 && cb < 14) {
      const int c = (wid - 2) * 64 + lane;
      lds_st<float>(M_B1 + c * 4, bflo(b1[128 * cb + c]) * LOG2E);
    }
    const int w = wid & 3, tt = rb * 4 + w;
    f32x16 a0 = {}, a1 = {};
    f32x4 cs = {};
    float sx = 0.f, sxx = 0.f;
    PIN(a0); PIN(a1); PIN(cs); SBAR();
    BAR();
    STAMP(9);
    if (wid < 4) {
      gemm_pass<0>(lane, w * 4096, 16384, w & 1, w >> 1, a0, a1, cs, sx, sxx);
      STAMP(1);
      cs_add(M_CSQK + (w & 1) * 128, cs, lane);
      BAR();
      tok = lds_tok();
      float rs = lds_ld<float>(M_RS + (32 * w + r) * 4), mr = lds_ld<float>(M_MR + (32 * w + r) * 4);
      if (cb < 8) { rs *= 0.25f * LOG2E; mr *= 0.25f * LOG2E; }
      const unsigned base = ((cb < 8) ? WS_Q : WS_K) + ((4 * (cb & 7)) * 64 + tt) * 1024u;
#pragma unroll
      for (int t = 0; t < 2; ++t) {
        const int ct = (w & 1) ^ t;
        const f32x16 a = t ? a1 : a0;
        float c[16];
        ld_rows(c, M_CSQK + 128 * ct, h);
        u32x4 p0, p1;
#pragma unroll
        for (int i = 0; i < 4; ++i) {
          p0[i] = pk2(a[2 * i] * rs - mr * c[2 * i], a[2 * i + 1] * rs - mr * c[2 * i + 1]);
          p1[i] = pk2(a[8 + 2 * i] * rs - mr * c[8 + 2 * i], a[9 + 2 * i] * rs - mr * c[9 + 2 * i]);
        }
        st_sc1(p0, rws, lane16, base + (2 * ct) * 65536u); st_sc1(p1, rws, lane16, base + (2 * ct + 1) * 65536u);
      }
    } else {
      gemm_pass<1>(lane, w * 4096, 16384 + 8192, cb & 1, w & 1, a0, a1, cs, sx, sxx);
      STAMP(1);
      sx = half_sum(sx); sxx = half_sum(sxx);
      const float mu = sx * (1.0f / 512.0f);
      const float var = fmaxf(sxx * (1.0f / 512.0f) - mu * mu, 0.f);
      const float rs = __builtin_amdgcn_rsqf(var + 1e-5f);
      if (lane < 32) { lds_st<float>(M_RS + (32 * w + r) * 4, rs); lds_st<float>(M_MR + (32 * w + r) * 4, mu * rs); }
      cs_add(M_CSV + (w >> 1) * 128, cs, lane);
      BAR();
      tok = lds_tok();
      float t0[16], t1[16];
      ld_rows(t0, M_RS + 128 * w, h); ld_rows(t1, M_MR + 128 * w, h);
      const float csv = lds_ld<float>(M_CSV + (w >> 1) * 128 + r * 4);
      u32x4 p0, p1;
#pragma unroll
      for (int i = 0; i < 4; ++i) {
        p0[i] = pk2(a0[2 * i] * t0[2 * i] - t1[2 * i] * csv, a0[2 * i + 1] * t0[2 * i + 1] - t1[2 * i + 1] * csv);
        p1[i] = pk2(a0[8 + 2 * i] * t0[8 + 2 * i] - t1[8 + 2 * i] * csv, a0[9 + 2 * i] * t0[9 + 2 * i] - t1[9 + 2 * i] * csv);
      }
      const unsigned vo = (r >> 4) * 65536 + h * 256 + (r & 15) * 16, so = WS_V + (2 * cb * 64 + tt) * 1024u;
      st_sc1(p0, rws, vo, so); st_sc1(p1, rws, vo + 512, so);
    }
    STAMP(2);
  }
  if (cb < 14) {
    const int rp = wid & 1, ct = wid >> 1;
    f32x16 a0 = {}, a1 = {};
    f32x4 cs = {};
    float sx, sxx;
    gemm_pass<2>(lane, rp * 8192, 16384 + (ct >> 1) * 8192, ct & 1, rp, a0, a1, cs, sx, sxx, ws, WS_QKFLAG + (cb * 16 + rb) * 8, tok);
    cs_add(M_CSF + ct * 128, cs, lane);
    BAR();
    float c[16], bb[16];
    ld_rows(c, M_CSF + ct * 128, h); ld_rows(bb, M_B1 + ct * 128, h);
#pragma unroll
    for (int i = 0; i < 2; ++i) {
      const float rs = lds_ld<float>(M_RS + (64 * rp + 32 * i + r) * 4) * LOG2E, mr = lds_ld<float>(M_MR + (64 * rp + 32 * i + r) * 4) * -LOG2E;
      const f32x16 a = i ? a1 : a0;
      unsigned p[8];
#pragma unroll
      for (int e = 0; e < 8; ++e) {
        const f32x2 off = __builtin_elementwise_fma((f32x2){c[2 * e], c[2 * e + 1]}, (f32x2){mr, mr}, (f32x2){bb[2 * e], bb[2 * e + 1]});
        const f32x2 m = mish2(__builtin_elementwise_fma((f32x2){a[2 * e], a[2 * e + 1]}, (f32x2){rs, rs}, off));
        p[e] = pk2(m[0], m[1]);
      }
      actp[2 * i] = (u32x4){p[0], p[1], p[2], p[3]}; actp[2 * i + 1] = (u32x4){p[4], p[5], p[6], p[7]};
    }
  } else {
    publish_wg(ws, WS_QKFLAG + (cb * 16 + rb) * 8, M_CNT, tok, lane);
    const int wgi = (cb - 14) * 16 + rb, q4 = (lane & 15) >> 2, ks = wid & 3, un = wid >> 2;
    const unsigned wl = 16384 + un * 8192 + ks * 2048 + (4 * h + q4) * 128 + ((lane >> 4) & 1) * 32 + (lane & 3) * 8 + ((q4 >> 1) << 6);
#pragma nounroll
    for (int i = 0; i < 8; ++i) {
      const int u = wgi + 32 * (i - 3);
      if (i >= 3 && u < 144) {
        const unsigned a = wl + (unsigned)(i & 3) * SLOT;
#pragma unroll
        for (int ct = 0; ct < 2; ++ct) {
          const unsigned aa = a ^ (ct << 6);
          const bf16x8 f = lds_tr2(aa, aa + 1024);
          st_sc1(__builtin_bit_cast(u32x4, f), rws, lane16, WS_W2F + ((4 * (u >> 2) + ks) * 16 + 4 * (u & 3) + 2 * un + ct) * 1024u);
        }
      }
      BAR();
    }
  }
  STAMP(3);

  f32x16 o = {};
  bf16x8 v0, v1 = {};
  u32x4 p0, p1 = {0, 0, 0, 0};
  PIN(o); PIN(v1); PIN(p1); SBAR();
  BAR_RAW();
  STAMP(4);
  if (cb < 14) {
#pragma unroll
    for (int i = 0; i < 2; ++i) {
      const unsigned so = WS_ACT + ((rb * 4 + 2 * (wid & 1) + i) * 112 + 8 * cb + 2 * (wid >> 1)) * 1024u;
      st_sc1(actp[2 * i], rws, lane16, so); st_sc1(actp[2 * i + 1], rws, lane16 + 1024, so);
    }
  }
  {
    const bf16x8 q = lds_ld<bf16x8>(L_Q + wid * 1024 + lane16);
    unsigned kp = L_K + lane16;
    const bool real = (lane & 16) == 0;
    const unsigned vp0 = (real ? L_V : L_ONES) + h * 256 + (lane & 15) * 16;
    unsigned vp = vp0;
    const unsigned vinc = real ? 4096u : 0u;
    const f32x16 zero = {};
    bf16x8 kn = lds_ld<bf16x8>(kp);
    f32x16 sA = MFMA32(kn, q, zero), sB;
    kn = lds_ld<bf16x8>(kp + 1024);
    float m = fmaxf(sA[0], sA[1]);
#pragma unroll
    for (int i = 2; i < 16; i += 2) m = fmaxf(fmaxf(m, sA[i]), sA[i + 1]);
    m = half_max(m);
    f32x16 negm;
#pragma unroll
    for (int i = 0; i < 16; ++i) { negm[i] = -m; sA[i] -= m; }
    PIN(negm);
    __builtin_amdgcn_s_setprio(2);
#define E2(S, i) do { S[i] = EX2(S[i]); S[i + 1] = EX2(S[i + 1]); } while (0)
#define STEP(SC, SN, u) do { \
    E2(SC, 0); PIN(SC); SBAR(); \
    o = MFMA32(v1, __builtin_bit_cast(bf16x8, p1), o); PIN(o); SBAR(); \
    v0 = lds_ld<bf16x8>(vp + (u) * 1024); v1 = lds_ld<bf16x8>(vp + (u) * 1024 + 512); SBAR(); \
    E2(SC, 2); E2(SC, 4); PIN(SC); SBAR(); \
    SN = MFMA32(kn, q, negm); PIN(SN); SBAR(); \
    kn = lds_ld<bf16x8>(kp + ((u) + 2) * 1024); SBAR(); \
    E2(SC, 6); E2(SC, 8); \
    p0[0] = pk2(SC[0], SC[1]); p0[1] = pk2(SC[2], SC[3]); p0[2] = pk2(SC[4], SC[5]); p0[3] = pk2(SC[6], SC[7]); PIN(p0); PIN(SC); SBAR(); \
    E2(SC, 10); PIN(SC); SBAR(); \
    o = MFMA32(v0, __builtin_bit_cast(bf16x8, p0), o); PIN(o); SBAR(); \
    E2(SC, 12); E2(SC, 14); \
    p1[0] = pk2(SC[8], SC[9]); p1[1] = pk2(SC[10], SC[11]); p1[2] = pk2(SC[12], SC[13]); p1[3] = pk2(SC[14], SC[15]); PIN(p1); SBAR(); \
  } while (0)
#pragma nounroll
    for (int it = 0; it < 16; ++it) {
      PIN(kp); PIN(vp);
      STEP(sA, sB, 0); STEP(sB, sA, 1); STEP(sA, sB, 2); STEP(sB, sA, 3);
      kp += 4096; vp += vinc;
      if ((0x480a >> it) & 1) {
        if (it == 1) BAR_RAW();
        else if (it == 3) publish_wg(ws, WS_ACTFLAG + (rb * 16 + cb) * 8, M_CNT2, tok, lane);
        else if (it == 11) __builtin_amdgcn_s_setprio(1);
        else __builtin_amdgcn_s_setprio(0);
      }
    }
#undef STEP
#undef E2
    o = MFMA32(v1, __builtin_bit_cast(bf16x8, p1), o);
    STAMP(5);
    float chk = o[8];
#pragma unroll
    for (int i = 0; i < 8; ++i) chk += fabsf(o[i]) * 0x1p-40f;
    if (__any(!(chk < 0x1p64f))) {
      float mm = -3.0e38f;
      o = zero;
#pragma nounroll
      for (int t = 0; t < 64; ++t) {
        f32x16 s = MFMA32(lds_ld<bf16x8>(L_K + lane16 + t * 1024), q, zero);
        float tm = s[0];
#pragma unroll
        for (int i = 1; i < 16; ++i) tm = fmaxf(tm, s[i]);
        const float mn = fmaxf(mm, half_max(tm));
        const float al = EX2(mm - mn);
        mm = mn;
        u32x4 pa, pb;
#pragma unroll
        for (int i = 0; i < 4; ++i) {
          pa[i] = pk2(EX2(s[2 * i] - mn), EX2(s[2 * i + 1] - mn));
          pb[i] = pk2(EX2(s[8 + 2 * i] - mn), EX2(s[9 + 2 * i] - mn));
        }
#pragma unroll
        for (int i = 0; i < 16; ++i) o[i] *= al;
        const unsigned va = vp0 + (real ? t * 1024 : 0);
        o = MFMA32(lds_ld<bf16x8>(va), __builtin_bit_cast(bf16x8, pa), o);
        o = MFMA32(lds_ld<bf16x8>(va + 512), __builtin_bit_cast(bf16x8, pb), o);
      }
    }
    const float rl = __builtin_amdgcn_rcpf(o[8]);
    const u32x4 ov = {pk2(o[0] * rl, o[1] * rl), pk2(o[2] * rl, o[3] * rl), pk2(o[4] * rl, o[5] * rl), pk2(o[6] * rl, o[7] * rl)};
    const int qt = qb * 8 + wid;
    st_sc1(ov, rws, lane16, WS_O + (qt * 32 + head) * 1024u);
    publish(ws, WS_OFLAG + (qt * 32 + head) * 8, tok, lane);
    STAMP(6);
    BAR();
  }
  {
    const int row = tid >> 3, c8 = tid & 7;
    const size_t gi = (size_t)(64 * om + row) * 512 + 64 * on + 8 * c8;
    const u32x4 xv = *(const u32x4*)(x + gi);
    BAR_RAW();
    STAMP(7);
    f32x4 s0 = {bflo(xv[0]), bfhi(xv[0]), bflo(xv[1]), bfhi(xv[1])}, s1 = {bflo(xv[2]), bfhi(xv[2]), bflo(xv[3]), bfhi(xv[3])};
    const unsigned pa = L_PART + row * PART_ROW + c8 * 32;
#pragma unroll
    for (int g = 0; g < 4; ++g) { s0 += lds_ld<f32x4>(pa + g * PART_SZ); s1 += lds_ld<f32x4>(pa + g * PART_SZ + 16); }
    { const rsrc_t ro = __builtin_amdgcn_make_buffer_rsrc(out, 0, 2048 * 512 * 2, 0x00020000);
      st_sc1((u32x4){pk2(s0[0], s0[1]), pk2(s0[2], s0[3]), pk2(s1[0], s1[1]), pk2(s1[2], s1[3])}, ro, (unsigned)gi * 2, 0); }
    if (blockIdx.x == 0 && tid == 0) __hip_atomic_store((gu64*)(ws + WS_EPOCH), tok, RLX_AGENT);
    STAMP(8);
    while (__builtin_amdgcn_s_memrealtime() < tpad + 5000) __builtin_amdgcn_s_sleep(1);
  }
}

extern "C" void kernel_launch(void* const* d_in, const int* in_sizes, int n_in, void* d_out, int out_size, void* d_ws,
                              size_t ws_size, hipStream_t stream) {
  fused_block<<<256, 768, 0, stream>>>((const uint16_t*)d_in[0], (const uint16_t*)d_in[1], (const uint16_t*)d_in[2],
                                       (const uint16_t*)d_in[4], (const uint16_t*)d_in[5], (const uint16_t*)d_in[6],
                                       (uint16_t*)d_out, (char*)d_ws);
}
```

```cpp
#include <hip/hip_runtime.h>
#include <stdint.h>

typedef __attribute__((ext_vector_type(8))) __bf16 bf16x8;
typedef __attribute__((ext_vector_type(16))) __bf16 bf16x16;
typedef __attribute__((ext_vector_type(2))) __bf16 bf16x2;
typedef __attribute__((ext_vector_type(2))) float f32x2;
typedef __attribute__((ext_vector_type(4))) float f32x4;
typedef __attribute__((ext_vector_type(16))) float f32x16;
typedef __attribute__((ext_vector_type(2))) unsigned u32x2;
typedef __attribute__((ext_vector_type(4))) unsigned u32x4;
typedef __attribute__((ext_vector_type(4))) short s16x4;
typedef unsigned long long u64;
typedef __attribute__((address_space(1))) u64 gu64;
typedef __amdgpu_buffer_rsrc_t rsrc_t;
#define AS3 __attribute__((address_space(3)))
#define DI __device__ __forceinline__

constexpr unsigned WS_EPOCH = 0;
constexpr unsigned FQ = 128, FA = 128, FO = 8;
constexpr unsigned WS_QKFLAG = 524288;
constexpr unsigned WS_ACTFLAG = WS_QKFLAG + 131072;
constexpr unsigned WS_OFLAG = WS_ACTFLAG + 32768;
constexpr unsigned WS_STAT = 65536;
constexpr unsigned WS_Q = 1u << 20;
constexpr unsigned WS_KV = WS_Q + (2u << 20);
constexpr unsigned WS_O = WS_KV + (4u << 20);
constexpr unsigned WS_ACT = WS_O + (2u << 20);
constexpr unsigned WS_W2F = WS_ACT + 64u * 112u * 1024u;
constexpr unsigned WS_CS = WS_W2F + 144u * 16u * 1024u;
constexpr unsigned WS_END = WS_CS + 32u * 16384u;

constexpr unsigned SLOT = 32768;
constexpr unsigned L_Q = 151552;
constexpr unsigned L_PART = 65536, PART_SZ = 16384;
constexpr unsigned L_MISC = 159744;
constexpr unsigned M_CSQK = L_MISC, M_CSV = M_CSQK + 256, M_CSF = M_CSV + 256,
                   M_GO = M_CSF + 512, M_CNT2 = M_GO + 12, M_DONE = M_GO + 16, M_KV = M_GO + 48, M_KV0 = M_GO + 80,
                   M_F = M_GO + 96, M_C = M_GO + 112, M_DR = M_GO + 128, M_CSOK = M_GO + 144, M_C2 = M_GO + 160,
                   M_TOK = L_MISC + 1280, M_B1RAW = M_TOK + 256, M_RS = L_MISC + 2048, M_MR = M_RS + 512, M_B1 = M_MR + 512;
constexpr unsigned L_XCH = 131072;
constexpr unsigned L_JUNK = 131072;
constexpr unsigned LDS_BYTES = 163840;
constexpr unsigned CODE_BYTES = 24 * 1024 + 512;

constexpr unsigned SPIN_CAP = 40000;
constexpr int PUBIT = 5, SW1 = 11, SW2 = 14;

#define MFMA32(a, b, c) __builtin_amdgcn_mfma_f32_32x32x16_bf16(a, b, c, 0, 0, 0)
#define MFMA16(a, b, c) __builtin_amdgcn_mfma_f32_16x16x32_bf16(a, b, c, 0, 0, 0)
#define SMPV(a, s, h, c, idx) __builtin_amdgcn_smfmac_f32_32x32x32_bf16(a, __builtin_bit_cast(bf16x16, __builtin_shufflevector(s, s, 8 * (h), 8 * (h) + 1, \
    8 * (h) + 2, 8 * (h) + 3, 8 * (h) + 4, 8 * (h) + 5, 8 * (h) + 6, 8 * (h) + 7)), c, idx, 0, 0)
#define SBAR() __builtin_amdgcn_sched_barrier(0)
#define PIN(x) asm volatile("" : "+v"(x))
#define BAR() asm volatile("s_waitcnt lgkmcnt(0)\n\ts_barrier" ::: "memory")
#define WAITL0() __builtin_amdgcn_s_waitcnt(0xC07F)
#define BAR_RAW() asm volatile("s_barrier" ::: "memory")
#define WAITV(n) asm volatile("s_waitcnt vmcnt(" #n ")" ::: "memory")
#define RLX_AGENT __ATOMIC_RELAXED, __HIP_MEMORY_SCOPE_AGENT
#define EX2(x) __builtin_amdgcn_exp2f(x)

template <typename T> DI T lds_ld(unsigned a) { return *(const AS3 T*)(uintptr_t)a; }
template <typename T> DI void lds_st(unsigned a, T v) { *(AS3 T*)(uintptr_t)a = v; }
DI bf16x8 lds_tr2(unsigned a0, unsigned a1) {
  s16x4 lo = __builtin_amdgcn_ds_read_tr16_b64_v4i16((AS3 s16x4*)(uintptr_t)a0);
  s16x4 hi = __builtin_amdgcn_ds_read_tr16_b64_v4i16((AS3 s16x4*)(uintptr_t)a1);
  return __builtin_bit_cast(bf16x8, __builtin_shufflevector(lo, hi, 0, 1, 2, 3, 4, 5, 6, 7));
}
DI unsigned pk2(float a, float b) { f32x2 v = {a, b}; return __builtin_bit_cast(unsigned, __builtin_convertvector(v, bf16x2)); }
DI unsigned pk2s(float a, float b) { unsigned r; asm("v_cvt_pk_bf16_f32 %0, %1, %2" : "=v"(r) : "v"(a), "v"(b)); return r; }
DI float lnf(float a, float sc, float m, float c) { return __builtin_fmaf(a, sc, -(m * c)); }
DI float bflo(unsigned u) { return __uint_as_float(u << 16); }
DI float bfhi(unsigned u) { return __uint_as_float(u & 0xffff0000u); }
DI float half_sum(float v) { auto r = __builtin_amdgcn_permlane32_swap(__float_as_uint(v), __float_as_uint(v), false, false); return __uint_as_float(r[0]) + __uint_as_float(r[1]); }
DI float half_max(float v) { auto r = __builtin_amdgcn_permlane32_swap(__float_as_uint(v), __float_as_uint(v), false, false); return fmaxf(__uint_as_float(r[0]), __uint_as_float(r[1])); }
template <int CTRL> DI float dpp_f(float v) { return __int_as_float(__builtin_amdgcn_update_dpp(0, __float_as_int(v), CTRL, 0xf, 0xf, true)); }
DI float row_sum(float v) { auto t = __builtin_amdgcn_permlane16_swap(__float_as_uint(v), __float_as_uint(v), false, false); return __uint_as_float(t[0]) + __uint_as_float(t[1]); }
DI float half_wave_sum(float v) {
  v += dpp_f<0xB1>(v); v += dpp_f<0x4E>(v); v += dpp_f<0x141>(v); v += dpp_f<0x140>(v);
  { auto t = __builtin_amdgcn_permlane16_swap(__float_as_uint(v), __float_as_uint(v), false, false); v = __uint_as_float(t[0]) + __uint_as_float(t[1]); }
  return v;
}
DI unsigned rfl(unsigned v) { return __builtin_amdgcn_readfirstlane(v); }
DI void dma4(rsrc_t r, const unsigned (&v)[4], unsigned soff, unsigned ldsdst) {
  asm volatile("s_mov_b32 m0, %0\n\ts_nop 0\n\tbuffer_load_dwordx4 %1, %5, %6 offen lds\n\tbuffer_load_dwordx4 %2, %5, %6 offen offset:1024 lds\n\t"
               "buffer_load_dwordx4 %3, %5, %6 offen offset:2048 lds\n\tbuffer_load_dwordx4 %4, %5, %6 offen offset:3072 lds"
               ::"s"(ldsdst), "v"(v[0]), "v"(v[1]), "v"(v[2]), "v"(v[3]), "s"(r), "s"(soff) : "memory");
}
DI void dma4_sc1(rsrc_t r, unsigned v, unsigned soff, unsigned ldsdst) {
  asm volatile("s_mov_b32 m0, %0\n\ts_nop 0\n\tbuffer_load_dwordx4 %1, %2, %3 offen sc1 lds\n\tbuffer_load_dwordx4 %1, %2, %3 offen offset:1024 sc1 lds\n\t"
               "buffer_load_dwordx4 %1, %2, %3 offen offset:2048 sc1 lds\n\tbuffer_load_dwordx4 %1, %2, %3 offen offset:3072 sc1 lds"
               ::"s"(ldsdst), "v"(v), "s"(r), "s"(soff) : "memory");
}
DI void st_sc1(u32x4 v, rsrc_t r, unsigned voff, unsigned soff) {
  __builtin_amdgcn_raw_buffer_store_b128(v, r, voff, soff, 16);
  asm volatile("s_nop 1" ::"v"(v) : "memory");
}
DI u32x4 ld_sc1(rsrc_t r, unsigned voff, unsigned soff) { return __builtin_amdgcn_raw_buffer_load_b128(r, voff, soff, 16); }
DI void publish(char* ws, unsigned off, u64 tok, int lane) {
  WAITV(0);
  if (lane == 0) __hip_atomic_store((gu64*)(ws + off), tok, RLX_AGENT);
}
DI void poll1(char* ws, unsigned off, u64 tok, bool slow = false) {
#pragma nounroll
  for (unsigned spins = 0; spins < SPIN_CAP; ++spins) {
    const u64 a = __hip_atomic_load((gu64*)(ws + off), RLX_AGENT);
    if (__all(a == tok)) break;
    if (slow) __builtin_amdgcn_s_sleep(24); else __builtin_amdgcn_s_sleep(2);
  }
  asm volatile("" ::: "memory");
}
DI unsigned lds_word(unsigned a) { return *(volatile AS3 unsigned*)(uintptr_t)a; }
DI void lds_sig(unsigned a, unsigned v) {
  asm volatile("" ::: "memory");
  *(volatile AS3 unsigned*)(uintptr_t)a = v;
  asm volatile("" ::: "memory");
}
template <int SLP = 1>
DI void lds_wait_ge(unsigned a, unsigned need) {
  unsigned v, n;
  asm volatile("s_mov_b32 %1, %4\n"
               "1:\n\tds_read_b32 %0, %2\n\ts_waitcnt lgkmcnt(0)\n\tv_cmp_gt_u32 vcc, %3, %0\n\ts_cbranch_vccz 2f\n\t"
               "s_sleep %5\n\ts_sub_u32 %1, %1, 1\n\ts_cbranch_scc0 1b\n2:"
               : "=&v"(v), "=&s"(n) : "v"(a), "s"(need), "i"(64 * SPIN_CAP), "i"(SLP) : "vcc", "scc", "memory");
}
DI void lds_wait_all(unsigned a, int slp) { if (slp == 1) lds_wait_ge<1>(a, 1); else lds_wait_ge<4>(a, 1); }
DI u64 lds_tok() { const u32x2 t = lds_ld<u32x2>(M_TOK); return (((u64)rfl(t[1]) << 32) | rfl(t[0])) + 1; }

DI void ld_rows(float (&d)[16], unsigned base) {
#pragma unroll
  for (int g = 0; g < 4; ++g) {
    const f32x4 v = lds_ld<f32x4>(base + 32 * g);
    d[4 * g] = v[0]; d[4 * g + 1] = v[1]; d[4 * g + 2] = v[2]; d[4 * g + 3] = v[3];
  }
}

DI void publish_wg(char* ws, unsigned off, unsigned cnt, u64 tok, int lane) {
  WAITV(0);
  if (lane == 0) {
    const unsigned old = __hip_atomic_fetch_add((AS3 unsigned*)(uintptr_t)cnt, 1u, __ATOMIC_RELAXED, __HIP_MEMORY_SCOPE_WORKGROUP);
    if (old == 3) __hip_atomic_store((gu64*)(ws + off), tok, RLX_AGENT);
  }
}

DI bf16x8 sel_frag(int lane) {
  const unsigned one2 = ((lane & 15) == ((lane >> 4) & 1)) ? 0x3f803f80u : 0u;
  return __builtin_bit_cast(bf16x8, (u32x4){one2, one2, one2, one2});
}
DI unsigned wfrag_addr(int lane, unsigned unit, int ct) {
  const int q4 = (lane & 15) >> 2;
  return unit + (8 * (lane >> 5) + q4) * 128 + ((lane >> 4) & 1) * 32 + (lane & 3) * 8 + ((ct ^ (q4 >> 1)) << 6);
}
DI void cs_read(bf16x8 (&f)[4], unsigned wa) {
#pragma unroll
  for (int j = 0; j < 4; ++j) f[j] = lds_tr2(wa + j * 2048, wa + j * 2048 + 512);
}
DI void cs_part(const bf16x8 (&f)[4], rsrc_t rws, unsigned dst, unsigned tag, int lane) {
  const bf16x8 sel = sel_frag(lane);
  f32x4 c = {};
#pragma unroll
  for (int j = 0; j < 4; ++j) c = MFMA16(sel, f[j], c);
  if (lane < 16) st_sc1((u32x4){__float_as_uint(c[0]), tag, __float_as_uint(c[1]), tag}, rws, lane * 16, dst);
}
DI bool cs_sum(const u32x4& a, const u32x4& b, unsigned tag, unsigned dst, int lane) {
  if (!__all(a[1] == tag && a[3] == tag && b[1] == tag && b[3] == tag)) return false;
  float s0 = __uint_as_float(a[0]) + __uint_as_float(b[0]), s1 = __uint_as_float(a[2]) + __uint_as_float(b[2]);
  PIN(s0);
  s0 = half_sum(row_sum(s0)); s1 = half_sum(row_sum(s1));
  if (lane < 32) lds_st<float>(dst + lane * 4, (lane & 16) ? s1 : s0);
  return true;
}
DI void cs_gather(rsrc_t rws, unsigned src, unsigned tag, unsigned dst, int lane) {
#pragma nounroll
  for (unsigned spins = 0; spins < SPIN_CAP; ++spins) {
    const u32x4 a = ld_sc1(rws, lane * 16, src), b = ld_sc1(rws, lane * 16 + 1024, src);
    if (cs_sum(a, b, tag, dst, lane)) break;
    __builtin_amdgcn_s_sleep(2);
    asm volatile("" ::: "memory");
  }
}
template <bool QKV, typename F>
DI void gemm_pass(int lane, unsigned xb, unsigned wb, int ct, int ctv, f32x16& a0, f32x16& a1, f32x16& a2, f32x16& a3, unsigned cw, F preload) {
  const int r = lane & 31, h = lane >> 5;
  unsigned fa = M_F + (lane & 3) * 4, fv;
  unsigned xa[4];
#pragma unroll
  for (int j = 0; j < 4; ++j) xa[j] = xb + r * 128 + (((h ^ ((r >> 1) & 7)) ^ (2 * j)) << 4);
  unsigned wA = wfrag_addr(lane, wb, ct), uA = wA ^ 64, vA = wfrag_addr(lane, wb + 8192, ctv);
  bf16x8 X[4], Z[4], W[4], U[4];
#define RX(j) X[j] = lds_ld<bf16x8>(xa[j])
#define RW(j) W[j] = lds_tr2(wA + (j) * 2048, wA + (j) * 2048 + 512)
#define RU(j) U[j] = lds_tr2(uA + (j) * 2048, uA + (j) * 2048 + 512)
#define RZ(j) do { if (QKV) Z[j] = lds_tr2(vA + (j) * 2048, vA + (j) * 2048 + 512); else Z[j] = lds_ld<bf16x8>(xa[j] + 4096); } while (0)
#define M0(j) do { a0 = MFMA32(W[j], X[j], a0); PIN(a0); SBAR(); } while (0)
#define M1(j) do { a1 = MFMA32(U[j], X[j], a1); PIN(a1); SBAR(); } while (0)
#define M2(j) do { if (QKV) a2 = MFMA32(X[j], Z[j], a2); else a2 = MFMA32(W[j], Z[j], a2); PIN(a2); SBAR(); } while (0)
#define M3(j) do { if (!QKV) { a3 = MFMA32(U[j], Z[j], a3); PIN(a3); SBAR(); } } while (0)
  PIN(xa[0]); PIN(xa[1]); PIN(xa[2]); PIN(xa[3]); PIN(wA); PIN(uA);
  PIN(a0); PIN(a1); PIN(a2); if (QKV) PIN(vA); else { PIN(a3); PIN(fa); PIN(cw); }
  SBAR();
  BAR();
  RX(0); RW(0); RU(0); RZ(0); RX(1); RW(1); RU(1); RZ(1); RX(2); RW(2); RU(2); RZ(2); RW(3);
  if (QKV) RU(3); else RX(3);
  SBAR();
#pragma nounroll
  for (int s = 0; s < 7; ++s) {
    const int d = (s == 3) ? -3 * (int)SLOT : (int)SLOT;
    M0(0); if (QKV) RX(3); else RU(3); RZ(3); if (!QKV) fv = lds_word(fa); SBAR();
    M1(0); if (!QKV) lds_sig(cw, 8 + s);
    xa[0] += d; xa[1] += d; xa[2] += d; xa[3] += d; PIN(xa[0]); PIN(xa[1]); PIN(xa[2]); PIN(xa[3]); SBAR();
    M2(0); wA += d; uA += d; PIN(wA); PIN(uA); if (QKV) { vA += d; PIN(vA); } SBAR();
    M3(0); M0(1); M1(1);
    if (QKV) {
      WAITL0(); SBAR();
      M2(1);
      BAR_RAW(); SBAR();
    } else {
      __builtin_amdgcn_s_waitcnt(0xC17F);
      SBAR();
      M2(1);
      const bool ok = __all(fv >= 9u + s);
      SBAR();
      M3(1);
      if (__builtin_expect(!ok, 0)) lds_wait_ge(fa, 9 + s);
      asm volatile("" ::: "memory");
      SBAR();
    }
    if (QKV) {
      M0(2); RX(0); RW(0); RU(0); SBAR(); M1(2); RZ(0); RX(1); RW(1); SBAR(); M2(2); RU(1); RZ(1); SBAR();
      M0(3); RX(2); RW(2); RU(2); SBAR(); M1(3); RZ(2); RW(3); SBAR(); M2(3); RU(3); SBAR();
    } else {
      M0(2); RX(0); RW(0); SBAR(); M1(2); RU(0); RZ(0); SBAR(); M2(2); RX(1); RW(1); SBAR(); M3(2); RU(1); RZ(1); SBAR();
      M0(3); RX(2); RW(2); SBAR(); M1(3); RU(2); RZ(2); SBAR(); M2(3); RX(3); RW(3); SBAR(); M3(3);
    }
  }
  M0(0); if (QKV) RX(3); else RU(3); RZ(3); SBAR();
  M1(0); M2(0); M3(0); M0(1); M1(1); M2(1); M3(1);
  WAITL0();
  if (QKV) BAR_RAW(); else lds_sig(cw, 15u);
  SBAR();
  if (QKV) { M2(2); preload(); SBAR(); M2(3); M0(2); M0(3); M1(2); M1(3); }
  else { M0(2); preload(); SBAR(); M0(3); M2(2); M2(3); M1(2); M1(3); M3(2); M3(3); }
#undef RX
#undef RW
#undef RU
#undef RZ
#undef M0
#undef M1
#undef M2
#undef M3
}
DI void mish_tile(const f32x16& a, float rs, float mr, const float (&c)[16], const float (&bb)[16], u32x4& o0, u32x4& o1) {
  f32x2 z[8], u[8];
#pragma unroll
  for (int e = 0; e < 8; ++e) z[e] = __builtin_elementwise_fma((f32x2){c[2 * e], c[2 * e + 1]}, (f32x2){mr, mr}, (f32x2){bb[2 * e], bb[2 * e + 1]});
  SBAR();
#pragma unroll
  for (int e = 0; e < 8; ++e) z[e] = __builtin_elementwise_fma((f32x2){a[2 * e], a[2 * e + 1]}, (f32x2){rs, rs}, z[e]);
  SBAR();
#pragma unroll
  for (int e = 0; e < 8; ++e) { u[e][0] = EX2(z[e][0]); u[e][1] = EX2(z[e][1]); }
  SBAR();
#pragma unroll
  for (int e = 0; e < 8; ++e) u[e] += 1.0f;
  SBAR();
#pragma unroll
  for (int e = 0; e < 8; ++e) u[e] = __builtin_elementwise_fma(u[e], u[e], (f32x2){1.0f, 1.0f});
  SBAR();
#pragma unroll
  for (int e = 0; e < 8; ++e) { u[e][0] = __builtin_amdgcn_rcpf(u[e][0]); u[e][1] = __builtin_amdgcn_rcpf(u[e][1]); }
  SBAR();
#pragma unroll
  for (int e = 0; e < 8; ++e) u[e] = __builtin_elementwise_fma(u[e], (f32x2){-1.3862943611198906f, -1.3862943611198906f}, (f32x2){0.6931471805599453f, 0.6931471805599453f});
  SBAR();
#pragma unroll
  for (int e = 0; e < 8; ++e) z[e] *= u[e];
  SBAR();
#pragma unroll
  for (int e = 0; e < 4; ++e) { o0[e] = pk2(z[e][0], z[e][1]); o1[e] = pk2(z[4 + e][0], z[4 + e][1]); }
  PIN(o0); PIN(o1);
  SBAR();
}

DI u32x4 pack_o(const f32x16& o) {
  const float rl = __builtin_amdgcn_rcpf(o[8]);
  u32x4 v;
#pragma unroll
  for (int i = 0; i < 4; ++i) v[i] = min(pk2(o[2 * i] * rl, o[2 * i + 1] * rl), 0xfffffffeu);
  return v;
}

__global__ __launch_bounds__(768, 3) void fused_block(const uint16_t* __restrict__ x, const uint16_t* __restrict__ wqkv,
                                                   const uint16_t* __restrict__ wo, const uint16_t* __restrict__ w1,
                                                   const uint16_t* __restrict__ b1, const uint16_t* __restrict__ w2,
                                                   uint16_t* __restrict__ out, char* ws) {
  __shared__ __attribute__((aligned(1024))) char smem[LDS_BYTES];
  asm volatile("" ::"v"((AS3 char*)smem) : "memory");
  const int tid = threadIdx.x, lane = tid & 63;
  const int wid = rfl(tid >> 6);
  u64 pc;
  asm volatile("s_getpc_b64 %0" : "=s"(pc));
  const rsrc_t rcode = __builtin_amdgcn_make_buffer_rsrc((void*)(pc & ~(u64)255), 0, CODE_BYTES, 0x00020000);
#define CODEPF(k) asm volatile("s_mov_b32 m0, %0\n\ts_nop 0\n\tbuffer_load_dwordx4 %1, %2, %3 offen lds" ::"s"(L_JUNK + 3072), "v"(lane * 16), "s"(rcode), "s"((((blockIdx.x >> 3) + (k)) & 31) * 1024))
  if (wid == 0) CODEPF(0);
  const int xcd = blockIdx.x & 7, jb = blockIdx.x >> 3;
  const int rb = (xcd >> 2) * 8 + (jb & 7), cb = (xcd & 3) * 4 + (jb >> 3);
  const int fb = ((cb & 7) == 7) ? 14 + (cb >> 3) : cb - (cb >> 3);
  const int head = ((xcd >> 1) + 4 * (xcd & 1)) * 4 + (jb & 3), qb = jb >> 2;
  const int om = xcd * 4 + (jb & 3), on = jb >> 2;
  const rsrc_t rws = __builtin_amdgcn_make_buffer_rsrc(ws, 0, WS_END, 0x00020000);
  const unsigned lane16 = lane * 16;
  const int g = wid & 3, un = g >> 1;
#define LANE_OFFSETS(l) \
  const unsigned vx0 = ((l) >> 3) * 1024 + ((((l) & 7) ^ ((l) >> 4)) << 4), vwc = (((l) & 7) ^ ((((l) >> 4) & 1) << 2)) << 4; \
  const unsigned vx[4] = {vx0, (vx0 ^ 64) + 7168u, vx0 + 2 * 7168u, (vx0 ^ 64) + 3 * 7168u}; \
  const unsigned vq0 = ((l) >> 3) * 3072u + vwc; \
  const unsigned vq[4] = {vq0, vq0 + 23552u, vq0 + 2 * 23552u, vq0 + 3 * 23552u}
  const unsigned wq = (un ? 2048u + 128u * (cb >> 1) : 128u * cb) + 32u * (g & 1) * 3072u;
  const int rot = rb;
  const unsigned xbase = (unsigned)(128 * rb + 32 * g) * 1024u, ldst = g * 4096;
#define rx __builtin_amdgcn_make_buffer_rsrc((void*)x, 0, 2048 * 512 * 2, 0x00020000)
#define rw __builtin_amdgcn_make_buffer_rsrc((void*)wqkv, 0, 512u * 3072u, 0x00020000)
#define XOFF(i) (xbase + 128u * (((i) + rot) & 7))
#define MXA(i) (ldst + ((i) & 3) * SLOT)
#define XG(i) do { dma4(rx, vx, XOFF(i), MXA(i)); } while (0)
#define QOFF(i) (wq + (((i) + rot) & 7) * (64u * 3072u))
#define QG(i) do { dma4(rw, vq, QOFF(i), MXA(i) + 16384); } while (0)
  {
    LANE_OFFSETS((unsigned)lane);
    int w8 = wid;
    asm volatile("" : "+s"(w8) : "v"(vx[0]), "v"(vx[1]), "v"(vx[2]), "v"(vx[3]), "v"(vq[0]), "v"(vq[1]), "v"(vq[2]), "v"(vq[3]), "s"(XOFF(0)), "s"(QOFF(0)), "s"(ldst));
    SBAR();
    asm volatile("" ::"s"(x), "s"(wqkv), "s"(wo), "s"(w1), "s"(b1), "s"(w2), "s"(out), "s"(ws));

    if (__builtin_expect(w8 >= 8, 1)) {
      XG(0); QG(0);
      asm volatile("s_mov_b32 m0, %0\n\ts_nop 0\n\tbuffer_load_dwordx4 %1, %2, %3 offen lds\n\tbuffer_load_dwordx4 %1, %2, %3 offen offset:1024 lds"
                   ::"s"(L_Q + g * 2048), "v"(lane16), "s"(rx), "s"((unsigned)(128 * rb + 8 * cb + 2 * g) * 1024u) : "memory");
      const rsrc_t rs = __builtin_amdgcn_make_buffer_rsrc((g & 1) ? (void*)b1 : (void*)ws, 0, 3584, 0x00020000);
      asm volatile("s_mov_b32 m0, %0\n\ts_nop 0\n\tbuffer_load_dword %1, %2, %3 offen sc1 lds"
                   ::"s"(M_TOK + (g & 1) * 256), "v"(lane * 4), "s"(rs), "s"((g & 1) ? 256u * fb : WS_EPOCH) : "memory");
      XG(1); QG(1);
    }
  }

  if (wid >= 8) {
    const unsigned cwa = M_C + (lane & 3) * 4;
    u64 tok;
    bf16x8 rf[5][4];
    {
      unsigned l2 = lane;
      PIN(l2);
      LANE_OFFSETS(l2);
      u32x4 park[4][4];
      unsigned pa = ldst + lane16, pb = pa + 2 * SLOT;
      PIN(pa); PIN(pb);
#define PADR(s, p) ((((s) & 2) ? pb : pa) + ((s) & 1) * SLOT + (p) * 1024)
#define PIN2(a, b) asm volatile("" ::"s"(a), "s"(b))
#define SYNC(s, WAIT) do { if ((s) < 5) WAITL0(); WAIT; SBAR(); \
        if ((s) < 4) { _Pragma("unroll") for (int p = 0; p < 4; ++p) park[(s) & 3][p] = lds_ld<u32x4>(PADR(s, p)); } \
        SBAR(); BAR_RAW(); } while (0)
#define P1(s) do { PIN2(XOFF((s) + 3), MXA((s) + 3)); PIN2(QOFF((s) + 2), MXA((s) + 2) + 16384); SYNC(s, WAITV(12)); QG((s) + 2); XG((s) + 3); } while (0)
      XG(2); P1(0); P1(1); P1(2); P1(3); P1(4);
#undef P1
      {
        const bool ff = fb < 14;
        const int wgi = (fb - 14) * 16 + rb;
        const unsigned stride = ff ? 3584u : 1024u, step = 8u * stride - 1024u;
        const unsigned vw0 = (lane >> 3) * stride + vwc, wb = 128u * un + 32u * (g & 1) * stride + (ff ? 256u * fb : (unsigned)(wgi & 3) * 256u);
        const unsigned vw[4] = {vw0, vw0 + step, vw0 + 2 * step, vw0 + 3 * step};
        const rsrc_t ra = __builtin_amdgcn_make_buffer_rsrc((void*)(ff ? w1 : w2), 0, 512u * 3584u, 0x00020000);
        const rsrc_t r11 = __builtin_amdgcn_make_buffer_rsrc((void*)(ff ? w1 : (wgi < 16) ? w2 : wo), 0, (ff || wgi < 16) ? 512u * 3584u : 512u * 1024u, 0x00020000);
#define UROW(i) ((unsigned)(((i) < 11 ? wgi + 32 * ((i) - 8) : (i) == 11 ? (wgi < 16 ? wgi + 96 : wgi - 16) : (wgi & 15) + 16) >> 2) * 65536u)
#define FOFF(i) (wb + (ff ? (((i) + 4 + rot) & 7) * (64u * 3584u) : UROW(i)))
#define P2(i, R, WAIT, PRE) do { const unsigned fo = FOFF(i); PIN2(fo, MXA(i) + 16384); SYNC((i) - 3, WAIT); PRE; \
        dma4(R, vw, fo, MXA(i) + 16384); \
        SBAR(); } while (0)
        P2(8, ra, WAITV(12), QG(7));
        P2(9, ra, WAITV(12), );
        tok = lds_tok();
        P2(10, ra, WAITV(8), ); P2(11, r11, if (ff) { WAITV(4); lds_sig(M_F + g * 4, 9u); }, );
#define P3(i) do { const unsigned fo = FOFF(i); PIN2(fo, MXA(i) + 16384); lds_wait_ge(cwa, (i) - 4); \
        dma4(ra, vw, fo, MXA(i) + 16384); \
        if ((i) > 12) { WAITV(4); lds_sig(M_F + g * 4, (i) - 1); } \
        { _Pragma("unroll") for (int p = 0; p < 4; ++p) lds_st<u32x4>(PADR(i, p), park[(i) & 3][p]); } \
        SBAR(); } while (0)
        if (ff) {
          WAITV(4);
          lds_sig(M_F + g * 4, 10u);
          WAITV(0);
          lds_sig(M_F + g * 4, 11u);
          lds_wait_all(M_C2 + (lane & 3) * 4, 1);
          P3(12); P3(13); P3(14); P3(15);
          WAITV(0);
          lds_sig(M_F + g * 4, 15u);
        } else {
          dma4(__builtin_amdgcn_make_buffer_rsrc((void*)wo, 0, 512u * 1024u, 0x00020000), vw, FOFF(12), MXA(0));
          WAITV(0);
          const int q4 = (lane & 15) >> 2;
          const unsigned wl = ldst + (4 * (lane >> 5) + q4) * 128 + ((lane >> 4) & 1) * 32 + (lane & 3) * 8 + ((q4 >> 1) << 6);
#pragma unroll
          for (int j = 0; j < 5; ++j)
#pragma unroll
            for (int e = 0; e < 4; ++e) {
              const unsigned aa = (wl ^ ((e & 1) << 6)) + (e >> 1) * 2048 + ((j < 4) ? j * SLOT + 16384 : 0);
              rf[j][e] = lds_tr2(aa, aa + 1024);
            }
          lds_sig(M_C + g * 4, 15u);
          lds_wait_all(M_DR + (lane & 3) * 4, 4);
        }
#undef P3
#undef P2
#undef FOFF
#undef UROW
      }
#undef QG
#undef QOFF
#undef SYNC
#undef PIN2
#undef XG
#undef MXA
#undef XOFF
#undef rx
#undef rw
#undef PADR
    }
    const unsigned hq = WS_Q + head * 65536u + qb * 8192u, hk = WS_KV + head * 131072u;
    __builtin_amdgcn_s_setprio(3);
    const int ps = lane >> 4;
    const unsigned pf = WS_QKFLAG + ((ps == 0) ? 8 + (head >> 2) : (ps == 1) ? (head >> 1) : (head >> 2)) * 16 * FQ;
    if (g == 0) {
      poll1(ws, pf + ((ps < 2) ? (lane & 3) : 2 * qb + (lane & 1)) * FQ, tok);
      lds_sig(M_GO, 1u);
    } else lds_wait_all(M_GO, 1);
    lds_wait_ge(cwa, 12);
#define KVG(i) do { dma4_sc1(rws, lane16, hk + (8 * (i) + g) * 4096, (8 * (i) + g) * 4096); dma4_sc1(rws, lane16, hk + (8 * (i) + 4 + g) * 4096, (8 * (i) + 4 + g) * 4096); } while (0)
    if (g < 2) dma4_sc1(rws, lane16, hq + g * 4096, L_Q + g * 4096);
    KVG(0);
    WAITV(0);
    lds_sig(M_KV0 + g * 4, 1u);
    {
      if (g == 0) {
        poll1(ws, pf + ((ps < 2) ? (lane & 15) : 2 * qb + (lane & 1)) * FQ, tok);
        lds_sig(M_GO + 4, 1u);
      } else lds_wait_all(M_GO + 4, 4);
    }
    lds_wait_ge<3>(cwa, 14);
    KVG(1); KVG(2);
    lds_wait_ge<3>(cwa, 15);
    KVG(3);
#undef KVG
    WAITV(0);
    lds_sig(M_KV + g * 4, 1u);
    if (fb >= 14) {
      const int wgi = (fb - 14) * 16 + rb;
#pragma unroll
      for (int j = 0; j < 5; ++j) {
        const int u = wgi + 32 * j;
        if (u < 144) {
#pragma unroll
          for (int e = 0; e < 4; ++e)
            st_sc1(__builtin_bit_cast(u32x4, rf[j][e]), rws, lane16, WS_W2F + ((4 * (u >> 2) + 2 * (g & 1) + (e >> 1)) * 16 + 4 * (u & 3) + 2 * un + (e & 1)) * 1024u);
        }
      }
      publish_wg(ws, WS_ACTFLAG + (rb * 16 + fb) * FA, M_CNT2, tok, lane);
    }
    {
      const int wg = (on >> 1) + 4 * (lane & 7);
      poll1(ws, WS_ACTFLAG + ((lane < 32) ? (om >> 1) * 16 + min(lane, 13) : (wg & 15) * 16 + 14 + (wg >> 4)) * FA, tok, true);
    }
    __builtin_amdgcn_s_setprio(0);
    f32x16 acc[2][2] = {};
    u32x4 wof[8][2], fa[4][2], fw[4][2];
    const unsigned gsw = ((xcd & 5) == 1);
#define GPH(j) ((j) ^ (((j) >> 1) & gsw))
#define F2MM(d) do { _Pragma("unroll") for (int i = 0; i < 2; ++i) _Pragma("unroll") for (int j = 0; j < 2; ++j) \
          acc[i][j] = MFMA32(__builtin_bit_cast(bf16x8, fw[d][j]), __builtin_bit_cast(bf16x8, fa[d][i]), acc[i][j]); SBAR(); } while (0)
    {
      const unsigned sa0 = WS_ACT + ((2 * om) * 112 + 28 * g) * 1024u, sa1 = sa0 + 112 * 1024u;
      const unsigned sw = WS_W2F + (28 * g * 16 + 2 * on) * 1024u;
      const unsigned so = WS_W2F + ((112 + g) * 16 + 2 * on) * 1024u;
#pragma unroll
      for (int d = 0; d < 4; ++d) {
        fa[d][0] = ld_sc1(rws, lane16, sa0 + d * 1024); fa[d][1] = ld_sc1(rws, lane16, sa1 + d * 1024);
        fw[d][0] = ld_sc1(rws, lane16, sw + d * 16384); fw[d][1] = ld_sc1(rws, lane16 + 1024, sw + d * 16384);
      }
      SBAR();
#pragma unroll
      for (int k = 0; k < 28; ++k) {
        const int d = k & 3;
        F2MM(d);
        if (k + 4 < 28) {
          fa[d][0] = ld_sc1(rws, lane16, sa0 + (k + 4) * 1024); fa[d][1] = ld_sc1(rws, lane16, sa1 + (k + 4) * 1024);
          fw[d][0] = ld_sc1(rws, lane16, sw + (k + 4) * 16384); fw[d][1] = ld_sc1(rws, lane16 + 1024, sw + (k + 4) * 16384);
        } else {
          const unsigned sd = so + GPH(d) * 131072;
          wof[2 * d][0] = ld_sc1(rws, lane16, sd); wof[2 * d][1] = ld_sc1(rws, lane16 + 1024, sd);
          wof[2 * d + 1][0] = ld_sc1(rws, lane16, sd + 65536); wof[2 * d + 1][1] = ld_sc1(rws, lane16 + 1024, sd + 65536);
        }
        SBAR();
      }
    }
    poll1(ws, WS_OFLAG + (om * 32 + g * 8 + (lane & 7)) * FO, tok, true);
    __builtin_amdgcn_s_setprio(3);
    lds_wait_ge<4>(M_DONE + (lane & 7) * 4, 1);
    if (xcd & 1) __builtin_amdgcn_s_sleep(3); else __builtin_amdgcn_s_sleep(15);
    {
      const unsigned so = WS_O + (om * 64 + g * 16) * 1024u, lo = g * 16384;
      unsigned ok = 0, la = lo + lane16;
      PIN(la);
      u32x4 ga[4], gb[4];
      unsigned mx;
#define WOSRC(j) (so + GPH(j) * 4096)
#define WORD(f, a) do { f[0] = lds_ld<u32x4>(a); f[1] = lds_ld<u32x4>((a) + 1024); f[2] = lds_ld<u32x4>((a) + 2048); f[3] = lds_ld<u32x4>((a) + 3072); } while (0)
#define WOMAX(f) max(max(max(max(f[0][0], f[0][1]), max(f[0][2], f[0][3])), max(max(f[1][0], f[1][1]), max(f[1][2], f[1][3]))), \
                     max(max(max(f[2][0], f[2][1]), max(f[2][2], f[2][3])), max(max(f[3][0], f[3][1]), max(f[3][2], f[3][3]))))
#define WOMM(f, j, q) do { acc[(q) & 1][0] = MFMA32(__builtin_bit_cast(bf16x8, wof[2 * (j) + ((q) >> 1)][0]), __builtin_bit_cast(bf16x8, f[q]), acc[(q) & 1][0]); \
                           acc[(q) & 1][1] = MFMA32(__builtin_bit_cast(bf16x8, wof[2 * (j) + ((q) >> 1)][1]), __builtin_bit_cast(bf16x8, f[q]), acc[(q) & 1][1]); \
                           PIN(acc[(q) & 1][0]); PIN(acc[(q) & 1][1]); SBAR(); } while (0)
#define WOGRP(f, n, j, NW) do { \
        if (__builtin_expect(__any(mx == 0xffffffffu), 0)) { \
          _Pragma("nounroll") for (unsigned spins = 0; spins < SPIN_CAP; ++spins) { \
            if ((j) < 3 && (spins & 1) == 0) {     \
              WAITV(0); \
              _Pragma("nounroll") for (unsigned k = (j) + 1; k < 4; ++k) { \
                if (ok & (1u << k)) continue; \
                WORD(f, la + k * 4096); \
                if (__any(WOMAX(f) == 0xffffffffu)) dma4_sc1(rws, lane16, WOSRC(k), lo + k * 4096); else ok |= 1u << k; } } \
            _Pragma("unroll") for (int q = 0; q < 4; ++q) f[q] = ld_sc1(rws, lane16, WOSRC(j) + q * 1024); \
            if (!__any(WOMAX(f) == 0xffffffffu)) break; \
            asm volatile("" ::: "memory"); } } \
        if ((j) == 3) break; \
        WOMM(f, j, 0); WOMM(f, j, 1); \
        if ((j) < 3) { WAITV(NW); WORD(n, la + ((j) + 1) * 4096); SBAR(); } \
        WOMM(f, j, 2); \
        acc[1][0] = MFMA32(__builtin_bit_cast(bf16x8, wof[2 * (j) + 1][0]), __builtin_bit_cast(bf16x8, f[3]), acc[1][0]); PIN(acc[1][0]); SBAR(); \
        if ((j) < 3) { mx = WOMAX(n); PIN(mx); SBAR(); } \
        acc[1][1] = MFMA32(__builtin_bit_cast(bf16x8, wof[2 * (j) + 1][1]), __builtin_bit_cast(bf16x8, f[3]), acc[1][1]); PIN(acc[1][1]); SBAR(); } while (0)
#pragma unroll
      for (int j = 0; j < 4; ++j) dma4_sc1(rws, lane16, WOSRC(j), lo + j * 4096);
      WAITV(12);
      WORD(ga, la); mx = WOMAX(ga);
      WOGRP(ga, gb, 0, 8); WOGRP(gb, ga, 1, 4); WOGRP(ga, gb, 2, 0); WOGRP(gb, ga, 3, 0);
#undef WOGRP
#undef WOMM
#undef WOMAX
#undef WORD
#undef WOSRC
      unsigned pb = L_PART + g * PART_SZ + (lane >> 5) * 8192 + (lane & 31) * 128 + (lane & 7) * 16;
      PIN(pb);
#define WOT(i, j) do { acc[i][j] = MFMA32(__builtin_bit_cast(bf16x8, wof[6][j]), __builtin_bit_cast(bf16x8, gb[i]), acc[i][j]); \
                       acc[i][j] = MFMA32(__builtin_bit_cast(bf16x8, wof[7][j]), __builtin_bit_cast(bf16x8, gb[2 + (i)]), acc[i][j]); PIN(acc[i][j]); SBAR(); } while (0)
#define WOS(i, j) do { _Pragma("unroll") for (int q = 0; q < 4; ++q) \
          lds_st<f32x4>((pb ^ ((4 * (j) + q) * 16)) + (i) * 4096, (f32x4){acc[i][j][4 * q], acc[i][j][4 * q + 1], acc[i][j][4 * q + 2], acc[i][j][4 * q + 3]}); SBAR(); } while (0)
      WOT(0, 0); WOT(0, 1); WOT(1, 0); WOS(0, 0); WOT(1, 1); WOS(0, 1); WOS(1, 0); WOS(1, 1);
#undef WOT
#undef WOS
    }
    BAR();
    return;
  }

  const int r = lane & 31, h = lane >> 5;
  u64 tok = 0;
  u32x4 actp[8];
  const float LOG2E = 1.4426950408889634f;
  const unsigned opair = qb * 256 + (head & 3) * 16 + (head >> 2) * 2;
  if (wid < 4) {
    const int w = wid, tt = rb * 4 + w;
    f32x16 a0 = {}, a1 = {}, a2 = {};
    float t0[16], t1[16], c0[16], c1[16], csv, rs, mr;
    u32x2 tk;
    bf16x8 cf[4];
    gemm_pass<true>(lane, w * 4096, 16384, 0, cb & 1, a0, a1, a2, a2, 0, [&]() __attribute__((always_inline)) {
      __builtin_amdgcn_s_setprio(3);
      unsigned mb = L_MISC + 16 * h, mc = L_MISC + r * 4;
      PIN(mb); PIN(mc);
      csv = lds_ld<float>(mc + (M_CSV - L_MISC));
      ld_rows(t1, mb + (M_MR - L_MISC) + 128 * w); ld_rows(t0, mb + (M_RS - L_MISC) + 128 * w);
      rs = lds_ld<float>(mc + (M_RS - L_MISC) + 128 * w); mr = lds_ld<float>(mc + (M_MR - L_MISC) + 128 * w);
    });
    {
      unsigned mb = L_MISC + 16 * h;
      PIN(mb);
      ld_rows(c0, mb); ld_rows(c1, mb + 128);
      tk = lds_ld<u32x2>(M_TOK);
      cs_read(cf, wfrag_addr(lane, 16384 + (w >> 1) * 8192, w & 1));
      lds_sig(M_C2 + w * 4, 1u);
      SBAR();
    }
    const unsigned qks = (cb < 8) ? 65536u : 131072u, qkb = (cb < 8) ? WS_Q + (4 * cb * 64 + tt) * 1024u : WS_KV + (4 * (cb - 8) * 64 + tt) * 2048u;
    {
      u32x4 p;
      const unsigned vo = (r >> 4) * 131072 + h * 512 + (r & 15) * 16, so = WS_KV + (2 * cb * 64 + tt) * 2048u + 1024u;
#pragma unroll
      for (int hf = 0; hf < 2; ++hf) {
#pragma unroll
        for (int i = 0; i < 4; ++i) { const int e = 8 * hf + 2 * i; p[i] = pk2s(lnf(a2[e], t0[e], t1[e], csv), lnf(a2[e + 1], t0[e + 1], t1[e + 1], csv)); }
        asm volatile("s_nop 1" : "+v"(p));
        { auto s0 = __builtin_amdgcn_permlane32_swap(p[0], p[2], false, false), s1 = __builtin_amdgcn_permlane32_swap(p[1], p[3], false, false);
          p = (u32x4){s0[0], s1[0], s0[1], s1[1]}; }
        st_sc1(p, rws, vo + 256 * hf, so); SBAR();
      }
      if (cb < 8) { rs *= 0.25f * LOG2E; mr *= 0.25f * LOG2E; }
#pragma unroll
      for (int hf = 0; hf < 4; ++hf) {
#pragma unroll
        for (int i = 0; i < 4; ++i) {
          const int e = 8 * (hf & 1) + 2 * i;
          p[i] = (hf < 2) ? pk2s(lnf(a0[e], rs, mr, c0[e]), lnf(a0[e + 1], rs, mr, c0[e + 1])) : pk2s(lnf(a1[e], rs, mr, c1[e]), lnf(a1[e + 1], rs, mr, c1[e + 1]));
        }
        st_sc1(p, rws, lane16, qkb + hf * qks); SBAR();
      }
    }
    tok = (((u64)rfl(tk[1]) << 32) | rfl(tk[0])) + 1;
    const unsigned csb = WS_CS + (cb * 2 + (rb >> 3)) * 16384u + (4 + w) * 2048u;
    cs_part(cf, rws, csb + (rb & 7) * 256, (unsigned)tok, lane);
    __builtin_amdgcn_s_setprio(0);
    WAITV(1);
    lds_sig(M_DR + w * 4, 1u);
    if (w == 3) {
      lds_wait_all(M_DR + (lane & 3) * 4, 1);
      if (lane == 0) __hip_atomic_store((gu64*)(ws + WS_QKFLAG + (cb * 16 + rb) * FQ), tok, RLX_AGENT);
    }
    lds_wait_ge<4>(M_C, 12);
    if (fb < 14) { cs_gather(rws, csb, (unsigned)tok, M_CSF + w * 128, lane); lds_sig(M_CSOK + w * 4, 1u); }
  } else {
    const int w = wid - 4;
    f32x16 a0 = {}, a1 = {}, a2 = {}, a3 = {};
    PIN(a0); PIN(a1); PIN(a2); PIN(a3);
    {
      gu64* sp = (gu64*)(ws + WS_STAT + ((128 * rb + 32 * w + r) * 2 + h) * 8);
      const unsigned csb = WS_CS + (cb * 2 + (rb >> 3)) * 16384u + w * 2048u;
      __builtin_amdgcn_s_setprio(1);
#define HSTAGE(s) do { SBAR(); BAR_RAW(); SBAR(); } while (0)
      if (w == 3 && lane < 11) lds_st<u32x4>(M_GO + lane16, (u32x4){0, 0, 0, 0});
      WAITL0();
      HSTAGE(0);
      float v[16], mu;
      bf16x8 f[4];
      unsigned bp;
      {
        const u32x4 xa = lds_ld<u32x4>(L_Q + (2 * w + h) * 1024 + r * 16), xb = lds_ld<u32x4>(L_Q + (2 * w + h) * 1024 + 512 + r * 16);
        const u32x2 tk = lds_ld<u32x2>(M_TOK);
        bp = lds_ld<unsigned>(M_B1RAW + lane * 4);
        cs_read(f, (w < 2) ? wfrag_addr(lane, 16384, w) : wfrag_addr(lane, 16384 + 8192, cb & 1));
        SBAR();
#pragma unroll
        for (int i = 0; i < 4; ++i) { v[2 * i] = bflo(xa[i]); v[2 * i + 1] = bfhi(xa[i]); v[8 + 2 * i] = bflo(xb[i]); v[9 + 2 * i] = bfhi(xb[i]); }
        float sm = v[0], sn = v[8];
#pragma unroll
        for (int i = 1; i < 8; ++i) { sm += v[i]; asm("v_add_f32 %0, %1, %2" : "=v"(sn) : "v"(sn), "v"(v[8 + i])); }
        mu = half_wave_sum(sm + sn) * (1.0f / 512.0f);
        tok = (((u64)rfl(tk[1]) << 32) | rfl(tk[0])) + 1;
        PIN(mu);
        WAITL0();
      }
      HSTAGE(1);
      {
        float q = 0.f, q2 = 0.f;
#pragma unroll
        for (int i = 0; i < 16; ++i) { asm("v_sub_f32 %0, %1, %2" : "=v"(v[i]) : "v"(v[i]), "v"(mu)); }
#pragma unroll
        for (int i = 0; i < 8; ++i) { q = __builtin_fmaf(v[i], v[i], q); asm("v_fma_f32 %0, %1, %1, %2" : "=v"(q2) : "v"(v[8 + i]), "v"(q2)); }
        const float rs = __builtin_amdgcn_rsqf(half_wave_sum(q + q2) * (1.0f / 512.0f) + 1e-5f);
        if (r < 2) __hip_atomic_store((gu64*)(ws + WS_STAT + ((128 * rb + 8 * cb + 2 * w + h) * 2 + r) * 8), (tok << 32) | __float_as_uint(r ? mu * rs : rs), RLX_AGENT);
      }
      HSTAGE(2);
      if (w < 3) cs_part(f, rws, csb + (rb & 7) * 256, (unsigned)tok, lane);
      else {
        float b0 = bflo(bp) * LOG2E; PIN(b0);
        lds_st<f32x2>(M_B1 + lane * 8, (f32x2){b0, bfhi(bp) * LOG2E});
      }
      HSTAGE(3); HSTAGE(4);
      HSTAGE(5);
      u64 gv = __hip_atomic_load(sp, RLX_AGENT);
      const u32x4 ca = ld_sc1(rws, lane16, csb), cc = ld_sc1(rws, lane16 + 1024, csb);
      HSTAGE(6);
      if (w < 3 && !cs_sum(ca, cc, (unsigned)tok, M_CSQK + w * 128, lane)) cs_gather(rws, csb, (unsigned)tok, M_CSQK + w * 128, lane);
      if (!__all((unsigned)(gv >> 32) == (unsigned)tok)) {
#pragma nounroll
        for (unsigned spins = 0; spins < SPIN_CAP; ++spins) {
          gv = __hip_atomic_load(sp, RLX_AGENT);
          if (__all((unsigned)(gv >> 32) == (unsigned)tok)) break;
          __builtin_amdgcn_s_sleep(1);
        }
      }
      lds_st<unsigned>((h ? M_MR : M_RS) + (32 * w + r) * 4, (unsigned)gv);
      HSTAGE(7);
#undef HSTAGE
      __builtin_amdgcn_s_setprio(3);
    }
    const int rp = w & 1, cp = w >> 1;
    if (fb < 14) {
      float rs[2], mr[2], c[16], bb[16];
      unsigned mb = L_MISC + 16 * h + (2 * cp + rp) * 128;
      gemm_pass<false>(lane, rp * 8192, 16384 + cp * 8192, rp, 0, a0, a1, a2, a3, M_C + w * 4, [&]() __attribute__((always_inline)) {
        lds_wait_all(M_CSOK + (2 * cp + (lane & 1)) * 4, 1);
        unsigned mc = L_MISC + (64 * rp + r) * 4;
        PIN(mb); PIN(mc);
#pragma unroll
        for (int i = 0; i < 2; ++i) { rs[i] = lds_ld<float>(mc + (M_RS - L_MISC) + 128 * i); mr[i] = lds_ld<float>(mc + (M_MR - L_MISC) + 128 * i); }
        ld_rows(c, mb + (M_CSF - L_MISC)); ld_rows(bb, mb + (M_B1 - L_MISC));
      });
#pragma unroll
      for (int i = 0; i < 2; ++i) { rs[i] *= LOG2E; mr[i] *= -LOG2E; }
      float c2[16], bb2[16];
      mb ^= 128;
      PIN(mb);
      ld_rows(c2, mb + (M_CSF - L_MISC)); ld_rows(bb2, mb + (M_B1 - L_MISC));
      SBAR();
      mish_tile(a0, rs[0], mr[0], c, bb, actp[0], actp[1]);
      mish_tile(a2, rs[1], mr[1], c, bb, actp[2], actp[3]);
      mish_tile(a1, rs[0], mr[0], c2, bb2, actp[4], actp[5]);
      mish_tile(a3, rs[1], mr[1], c2, bb2, actp[6], actp[7]);
    } else {
      BAR();
    }
  }

  const unsigned vpad = ((lane & 27) == 16) ? 0x3f803f80u : 0u;
  bf16x8 vB1 = __builtin_bit_cast(bf16x8, (u32x4){vpad, vpad, vpad, vpad}), vA0 = vB1, vA1 = vB1, vB0 = vB1;
  u32x4 p1 = {0, 0, 0, 0};
  int sidx = 0xdddd;
  PIN(vB1); PIN(vA0); PIN(vA1); PIN(vB0); PIN(p1); PIN(sidx); SBAR();
  f32x16 oa = MFMA32(vB1, __builtin_bit_cast(bf16x8, p1), (f32x16){});
  PIN(oa); PIN(p1); SBAR();
  f32x16 ob = MFMA32(vB1, __builtin_bit_cast(bf16x8, p1), (f32x16){});
  PIN(ob); SBAR();
  lds_wait_all(M_KV0 + (lane & 3) * 4, 1);
  const unsigned oslot = opair + g * 64;
  {
    const bf16x8 qa = lds_ld<bf16x8>(L_Q + g * 2048 + lane16), qb2 = lds_ld<bf16x8>(L_Q + g * 2048 + 1024 + lane16);
    unsigned kp = lane16 + (wid >> 2) * 2048;
#define RDKV(V0, V1, K, t) asm volatile("s_mov_b64 exec, %4\n\tds_read_b128 %0, %3 offset:%c5\n\tds_read_b128 %1, %3 offset:%c6\n\ts_mov_b64 exec, -1\n\t" \
    "ds_read_b128 %2, %3 offset:%c7" : "+v"(V0), "+v"(V1), "=v"(K) : "v"(kp), "s"(vmask), "i"((t) * 4096 + 1024), "i"((t) * 4096 + 1280), "i"((t) * 4096 + 4096))
    u64 vmask = 0x0000ffff0000ffffull;
    asm volatile("" : "+s"(vmask));
    const f32x16 zero = {};
    const bf16x8 k0 = lds_ld<bf16x8>(lane16);
    bf16x8 kA, kB = lds_ld<bf16x8>(kp);
    RDKV(vA0, vA1, kA, 0);
    f32x16 sA = MFMA32(k0, qa, zero), sB = MFMA32(k0, qb2, zero);
    float m = __builtin_fmaxf(sA[0], sA[1]), m2 = __builtin_fmaxf(sB[0], sB[1]);
#pragma unroll
    for (int i = 2; i < 16; i += 2) { m = __builtin_fmaxf(__builtin_fmaxf(m, sA[i]), sA[i + 1]); m2 = __builtin_fmaxf(__builtin_fmaxf(m2, sB[i]), sB[i + 1]); }
    m = half_max(__builtin_fmaxf(m, m2));
    f32x16 negm;
#pragma unroll
    for (int i = 0; i < 16; ++i) negm[i] = -m;
    PIN(negm);
    sA = MFMA32(kB, qa, negm);
    PIN(p1);
    sB = MFMA32(vB1, __builtin_bit_cast(bf16x8, p1), zero);
    if (wid < 4) __builtin_amdgcn_s_setprio(2); else __builtin_amdgcn_s_setprio(3);
    const unsigned evt = 4u | (1u << PUBIT) | ((wid >= 4) ? (1u << SW1) : (1u << SW2));
#define E1(S, i) S[i] = EX2(S[i])
#define STEP(SC, SN, OP, VP, KN, QN, OC, VC, MID) do { \
    E1(SC, 0); E1(SC, 1); PIN(SC); SBAR(); \
    OP = SMPV(VP, SN, 1, OP, sidx); PIN(OP); SBAR(); \
    E1(SC, 2); E1(SC, 3); E1(SC, 4); PIN(SC); SBAR(); \
    MID; SBAR(); \
    SN = MFMA32(KN, QN, negm); PIN(SN); SBAR(); \
    E1(SC, 5); E1(SC, 6); E1(SC, 7); SBAR(); E1(SC, 8); E1(SC, 9); E1(SC, 10); SBAR(); \
    OC = SMPV(VC, SC, 0, OC, sidx); PIN(OC); SBAR(); \
    E1(SC, 11); E1(SC, 12); E1(SC, 13); E1(SC, 14); E1(SC, 15); PIN(SC); SBAR(); \
  } while (0)
#define TILE(VC0, VC1, KC, VN0, VN1, KN, u) do { \
    STEP(sA, sB, ob, VN1, KN, qb2, oa, VC0, asm volatile("s_waitcnt lgkmcnt(0)")); \
    STEP(sB, sA, oa, VC1, KC, qa, ob, VC0, RDKV(VN0, VN1, KN, (u) + 1)); } while (0)
#pragma nounroll
    for (int it = 0; it < 16; ++it) {
      TILE(vA0, vA1, kA, vB0, vB1, kB, 0); TILE(vB0, vB1, kB, vA0, vA1, kA, 1);
      kp += 8192;
      if (__builtin_expect((evt >> it) & 1, 0)) {
        if (it == 2) {
          lds_wait_all(M_KV + (lane & 3) * 4, 1);
          if (wid < 4) {
            st_sc1((u32x4){~0u, ~0u, ~0u, ~0u}, rws, lane16, WS_O + oslot * 1024u); st_sc1((u32x4){~0u, ~0u, ~0u, ~0u}, rws, lane16 + 1024, WS_O + oslot * 1024u);
          } else if (fb < 14) {
            const int rp = wid & 1, cp = (wid - 4) >> 1;
#pragma unroll
            for (int j = 0; j < 2; ++j)
#pragma unroll
              for (int i = 0; i < 2; ++i) {
                const unsigned so = WS_ACT + ((rb * 4 + 2 * rp + i) * 112 + 8 * fb + 2 * (2 * cp + (rp ^ j))) * 1024u;
                st_sc1(actp[(j * 2 + i) * 2], rws, lane16, so); st_sc1(actp[(j * 2 + i) * 2 + 1], rws, lane16 + 1024, so);
              }
          }
        } else if (it == PUBIT) {
          if (wid < 4) publish(ws, WS_OFLAG + oslot / 2 * FO, tok, lane);
          else if (fb < 14) publish_wg(ws, WS_ACTFLAG + (rb * 16 + fb) * FA, M_CNT2, tok, lane);
        } else if (wid >= 4) __builtin_amdgcn_s_setprio(1);
        else __builtin_amdgcn_s_setprio(0);
      }
    }
#undef TILE
#undef STEP
#undef E1
#undef RDKV
    __builtin_amdgcn_s_setprio(3);
    unsigned xa = L_XCH + g * 5120 + lane16;
    PIN(xa);
    const unsigned pdone = lds_word(M_DONE + (g + 4) * 4);
    asm volatile("" ::: "memory");
    f32x4 t0 = lds_ld<f32x4>(xa), t1 = lds_ld<f32x4>(xa + 1024), t2 = lds_ld<f32x4>(xa + 2048), t3 = lds_ld<f32x4>(xa + 3072);
    f32x2 t4 = lds_ld<f32x2>(xa + 4096);
    SBAR();
    ob = SMPV(vB1, sB, 1, ob, sidx);
    PIN(ob); SBAR();
    asm volatile("s_waitcnt lgkmcnt(0)" : "+v"(vA0), "+v"(vA1), "+v"(kA));
    if (wid >= 4) {
      lds_st<f32x4>(xa, (f32x4){oa[0], oa[1], oa[2], oa[3]}); lds_st<f32x4>(xa + 1024, (f32x4){oa[4], oa[5], oa[6], oa[7]});
      lds_st<f32x4>(xa + 2048, (f32x4){ob[0], ob[1], ob[2], ob[3]}); lds_st<f32x4>(xa + 3072, (f32x4){ob[4], ob[5], ob[6], ob[7]});
      lds_st<f32x2>(xa + 4096, (f32x2){oa[8], ob[8]});
      lds_sig(M_DONE + wid * 4, 1u);
      __builtin_amdgcn_s_setprio(0);
    } else {
      if (!rfl(pdone)) {
        lds_wait_all(M_DONE + (g + 4) * 4, 1);
        t0 = lds_ld<f32x4>(xa); t1 = lds_ld<f32x4>(xa + 1024); t2 = lds_ld<f32x4>(xa + 2048); t3 = lds_ld<f32x4>(xa + 3072);
        t4 = lds_ld<f32x2>(xa + 4096);
      }
#pragma unroll
      for (int i = 0; i < 4; ++i) { oa[i] += t0[i]; oa[4 + i] += t1[i]; ob[i] += t2[i]; ob[4 + i] += t3[i]; }
      oa[8] += t4[0]; ob[8] += t4[1];
      f32x2 nf = {0.f, 0.f}, ng = {0.f, 0.f};
#pragma unroll
      for (int i = 0; i < 8; i += 2) {
        nf = __builtin_elementwise_fma((f32x2){oa[i], oa[i + 1]}, (f32x2){0.f, 0.f}, nf);
        ng = __builtin_elementwise_fma((f32x2){ob[i], ob[i + 1]}, (f32x2){0.f, 0.f}, ng);
      }
      nf += ng;
      const float lmin = fminf(oa[8], ob[8]) + nf[0], lsum = (oa[8] + ob[8]) + nf[1];
      u32x4 ova = pack_o(oa), ovb = pack_o(ob);
      if (__builtin_expect(__any(!(lmin > 0x1p-64f && lsum < 0x1p64f)), 0)) {
#pragma nounroll
        for (int x2 = 0; x2 < 2; ++x2) {
          const bf16x8 q = x2 ? qb2 : qa;
          float mm = -3.0e38f;
          f32x16 o = zero;
#pragma nounroll
          for (int t = 0; t < 64; ++t) {
            f32x16 sc = MFMA32(lds_ld<bf16x8>(lane16 + t * 2048), q, zero);
            float tm = sc[0];
#pragma unroll
            for (int i = 1; i < 16; ++i) tm = fmaxf(tm, sc[i]);
            const float mn = fmaxf(mm, half_max(tm));
            const float al = EX2(mm - mn);
            mm = mn;
#pragma unroll
            for (int i = 0; i < 16; ++i) sc[i] = EX2(sc[i] - mn);
#pragma unroll
            for (int i = 0; i < 16; ++i) o[i] *= al;
            u32x4 va = lds_ld<u32x4>(lane16 + t * 2048 + 1024), vb = lds_ld<u32x4>((lane16 ^ 256) + t * 2048 + 1024);
            if (lane16 & 256) { va = (u32x4){vpad, vpad, vpad, vpad}; vb = va; }
            o = SMPV(__builtin_bit_cast(bf16x8, va), sc, 0, o, sidx);
            o = SMPV(__builtin_bit_cast(bf16x8, vb), sc, 1, o, sidx);
          }
          if (x2) ovb = pack_o(o); else ova = pack_o(o);
        }
      }
      st_sc1(ova, rws, lane16, WS_O + oslot * 1024u);
      st_sc1(ovb, rws, lane16 + 1024, WS_O + oslot * 1024u);
      lds_sig(M_DONE + wid * 4, 1u);
    }
  }
  {
    const int row = tid >> 3, c8 = tid & 7;
    const size_t gi = (size_t)(64 * om + row) * 512 + 64 * on + 8 * c8;
    const u32x4 xv = *(const u32x4*)(x + gi);
    f32x4 s0 = {bflo(xv[0]), bfhi(xv[0]), bflo(xv[1]), bfhi(xv[1])}, s1 = {bflo(xv[2]), bfhi(xv[2]), bflo(xv[3]), bfhi(xv[3])};
    unsigned pa = L_PART + row * 128 + ((c8 ^ (row & 7)) << 4), go = (unsigned)gi * 2;
    PIN(s0); PIN(s1); PIN(pa); PIN(go); SBAR();
    BAR_RAW();
    {
      f32x4 t[8];
#pragma unroll
      for (int k = 0; k < 8; ++k) t[k] = lds_ld<f32x4>(pa + k * 8192);
      s0 = (s0 + (t[0] + t[2])) + (t[4] + t[6]); s1 = (s1 + (t[1] + t[3])) + (t[5] + t[7]);
    }
    __builtin_amdgcn_raw_buffer_store_b128((u32x4){pk2(s0[0], s0[1]), pk2(s0[2], s0[3]), pk2(s1[0], s1[1]), pk2(s1[2], s1[3])},
                                           __builtin_amdgcn_make_buffer_rsrc(out, 0, 2048 * 512 * 2, 0x00020000), go, 0, 2);
    if (blockIdx.x == 0 && tid == 0) __hip_atomic_store((gu64*)(ws + WS_EPOCH), tok, RLX_AGENT);
  }
}

extern "C" void kernel_launch(void* const* d_in, const int* in_sizes, int n_in, void* d_out, int out_size, void* d_ws,
                              size_t ws_size, hipStream_t stream) {
  fused_block<<<256, 768, 0, stream>>>((const uint16_t*)d_in[0], (const uint16_t*)d_in[1], (const uint16_t*)d_in[2],
                                       (const uint16_t*)d_in[4], (const uint16_t*)d_in[5], (const uint16_t*)d_in[6],
                                       (uint16_t*)d_out, (char*)d_ws);
}
```
